```python
import torch

def _softmax(x, axis=-1):
    return torch.softmax(x, dim=axis)
def _gelu(x):
    return torch.nn.functional.gelu(x, approximate="tanh")
def _silu(x):
    return torch.nn.functional.silu(x)
def _sigmoid(x):
    return torch.sigmoid(x)
def _layernorm(x, axis=-1, eps=1e-5):
    m = x.mean(dim=axis, keepdim=True)
    v = x.var(dim=axis, keepdim=True, unbiased=False)
    return (x - m) / torch.sqrt(v + eps)
def _scatter_add(tgt, idx, src, axis=0):
    return tgt.clone().index_add_(axis, idx, src)
def _segment_sum(src, seg, n):
    out = torch.zeros((n,) + src.shape[1:], dtype=src.dtype)
    return out.index_add_(0, seg, src)
def _pad(x, pw):
    flat = tuple(v for pair in reversed(pw) for v in pair)
    return torch.nn.functional.pad(x, flat)

kernel_name = 'gated_mix__sliding_block__mixer_s_mlp__prompt'

def setup_inputs():

    _w = (lambda a: (lambda t: t.to(torch.bfloat16) if t.is_floating_point() else t)(torch.as_tensor(a)))
    def _A_setup():
        B, S, D = 1, 2048, 448
        return {
            "x": _w((lambda *s: torch.randn(*s, dtype=torch.bfloat16) * 0.05)(B, S, D)),
            "wqkv": _w((lambda *s: torch.randn(*s, dtype=torch.bfloat16) * 3.2)(D, 3 * D)),
            "wo": _w((lambda *s: torch.randn(*s, dtype=torch.bfloat16) * 0.05)(D, D)),
        }
    _A_in = _A_setup()

    def _B_setup():
        B, S, D, F = 1, 2048, 448, 1792
        return {
            "x":  _w((lambda *s: torch.randn(*s, dtype=torch.bfloat16) * 0.05)(B, S, D)),
            "w1": _w((lambda *s: torch.randn(*s, dtype=torch.bfloat16) * 3.2)(D, D) * (D ** -0.5)),
            "w2": _w((lambda *s: torch.randn(*s, dtype=torch.bfloat16) * 3.2)(D, D) * (D ** -0.5)),
            "w3": _w((lambda *s: torch.randn(*s, dtype=torch.bfloat16) * 3.2)(D, F) * (D ** -0.5)),
            "w4": _w((lambda *s: torch.randn(*s, dtype=torch.bfloat16) * 3.2)(F, D) * (F ** -0.5)),
        }
    _B_in = _B_setup()

    return {
        "A_x": _A_in["x"],
        "A_wqkv": _A_in["wqkv"],
        "A_wo": _A_in["wo"],
        "B_x": _B_in["x"],
        "B_w1": _B_in["w1"],
        "B_w2": _B_in["w2"],
        "B_w3": _B_in["w3"],
        "B_w4": _B_in["w4"],
        "gate_w": _w((lambda *s: torch.randn(*s, dtype=torch.bfloat16) * 0.05)(448) * 16.0),
    }

def reference(A_x, A_wqkv, A_wo, B_x, B_w1, B_w2, B_w3, B_w4, gate_w):
    def _A_ref(x, wqkv, wo):
        B, S, D = x.shape
        H = 16; Dh = D // H; W = 256
        qkv = (lambda x, *d: x.permute(*d))((x @ wqkv).reshape(B, S, 3, H, Dh), 2, 0, 3, 1, 4)
        q, k, v = qkv[0], qkv[1], qkv[2]
        scores = torch.einsum("bhqd,bhkd->bhqk", q, k) * (Dh ** -0.5)
        idx = torch.arange(S)
        mask = (idx[:, None] - idx[None, :] >= 0) & (idx[:, None] - idx[None, :] < W)
        scores = torch.where(mask[None, None], scores, -1e9)
        p = _softmax(scores, axis=-1)
        o = torch.einsum("bhqk,bhkd->bhqd", p, v)
        return (lambda x, *d: x.permute(*d))(o, 0, 2, 1, 3).reshape(B, S, D) @ wo

    def _B_ref(x, w1, w2, w3, w4):
        B, S, D = x.shape
        h = _gelu(_layernorm(x, axis=-1) @ w1)
        c = D // 4
        g0 = _pad(h[:, :, 0*c:1*c], ((0, 0), (1, 0), (0, 0)))[:, :S, :]
        g1 = _pad(h[:, :, 1*c:2*c], ((0, 0), (0, 1), (0, 0)))[:, 1:, :]
        g2 = _pad(h[:, :, 2*c:3*c], ((0, 0), (2, 0), (0, 0)))[:, :S, :]
        g3 = _pad(h[:, :, 3*c:   ], ((0, 0), (0, 2), (0, 0)))[:, 2:, :]
        hs = torch.concatenate([g0, g1, g2, g3], axis=-1) @ w2
        x1 = x + hs
        h2 = _gelu(_layernorm(x1, axis=-1) @ w3) @ w4
        return x1 + h2

    x = A_x
    g = _sigmoid((x * gate_w).sum(-1, keepdims=True))
    a = _A_ref(x, A_wqkv, A_wo)
    b = _B_ref(x, B_w1, B_w2, B_w3, B_w4)
    return g * a + (1 - g) * b
```

```cpp
#include <hip/hip_runtime.h>
#include <hip/hip_bf16.h>
typedef __hip_bfloat16 bf16;
#define S_ 2048
#define D_ 448
#define F_ 1792
__device__ inline float b2f(bf16 v){ return __bfloat162float(v); }
__device__ inline float gelu(float x){ float u = 0.7978845608028654f*(x+0.044715f*x*x*x); return 0.5f*x*(1.f+tanhf(u)); }

__global__ void gemm_naive(const float* A, const bf16* B, float* C, int M, int N, int K, int act, int rnd){
  int n = blockIdx.x*64+threadIdx.x; int m = blockIdx.y;
  if(n>=N) return;
  float acc=0.f;
  for(int k=0;k<K;++k) acc += A[(size_t)m*K+k]*b2f(B[(size_t)k*N+n]);
  if(act) acc=gelu(acc);
  if(rnd) acc=b2f(__float2bfloat16(acc));
  C[(size_t)m*N+n]=acc;
}
__global__ void prep(const bf16* x, const bf16* gw, float* xf, float* lnx, float* g){
  int m=blockIdx.x; __shared__ float red[3][64];
  float s=0,gs=0; for(int k=threadIdx.x;k<D_;k+=64){ float v=b2f(x[m*D_+k]); xf[m*D_+k]=v; s+=v; gs+=v*b2f(gw[k]); }
  for(int o=32;o;o>>=1){ s+=__shfl_xor(s,o); gs+=__shfl_xor(gs,o);}
  float mean=s/D_; float q=0; for(int k=threadIdx.x;k<D_;k+=64){ float v=b2f(x[m*D_+k])-mean; q+=v*v; }
  for(int o=32;o;o>>=1) q+=__shfl_xor(q,o);
  float rstd=rsqrtf(q/D_+1e-5f);
  for(int k=threadIdx.x;k<D_;k+=64) lnx[m*D_+k]=(b2f(x[m*D_+k])-mean)*rstd;
  if(threadIdx.x==0) g[m]=1.f/(1.f+expf(-gs));
}
__global__ void ln(const float* x, float* y){
  int m=blockIdx.x;
  float s=0; for(int k=threadIdx.x;k<D_;k+=64) s+=x[m*D_+k];
  for(int o=32;o;o>>=1) s+=__shfl_xor(s,o);
  float mean=s/D_; float q=0; for(int k=threadIdx.x;k<D_;k+=64){ float v=x[m*D_+k]-mean; q+=v*v; }
  for(int o=32;o;o>>=1) q+=__shfl_xor(q,o);
  float rstd=rsqrtf(q/D_+1e-5f);
  for(int k=threadIdx.x;k<D_;k+=64) y[m*D_+k]=(x[m*D_+k]-mean)*rstd;
}
__global__ void shiftk(const float* h, float* hs){
  int m=blockIdx.x;
  for(int k=threadIdx.x;k<D_;k+=64){ int g=k/112; int sh = g==0?-1: g==1?1: g==2?-2:2; int t=m+sh; hs[m*D_+k]=(t>=0&&t<S_)?h[t*D_+k]:0.f; }
}
__global__ void addk(const float* a, const float* b, float* c, int n){ int i=blockIdx.x*256+threadIdx.x; if(i<n) c[i]=b2f(__float2bfloat16(a[i]+b[i])); }
__global__ void attn(const float* qkv, float* o){
  int t=blockIdx.x, h=blockIdx.y; int lane=threadIdx.x;
  __shared__ float p[256]; __shared__ float red[4];
  int key=t-255+lane; float s=-INFINITY;
  if(key>=0){ s=0; for(int d=0;d<28;++d) s+=qkv[(size_t)t*1344+h*28+d]*qkv[(size_t)key*1344+448+h*28+d]; s*=0.1889822365046136f; }
  float m=s; for(int o2=32;o2;o2>>=1) m=fmaxf(m,__shfl_xor(m,o2));
  if((lane&63)==0) red[lane>>6]=m; __syncthreads(); m=fmaxf(fmaxf(red[0],red[1]),fmaxf(red[2],red[3])); __syncthreads();
  float e= key>=0? expf(s-m):0.f; p[lane]=e; float l=e; for(int o2=32;o2;o2>>=1) l+=__shfl_xor(l,o2);
  if((lane&63)==0) red[lane>>6]=l; __syncthreads(); l=red[0]+red[1]+red[2]+red[3];
  if(lane<28){ float acc=0; for(int j=0;j<256;++j){ int kk=t-255+j; if(kk>=0) acc+=p[j]*qkv[(size_t)kk*1344+896+h*28+lane]; } o[t*D_+h*28+lane]=b2f(__float2bfloat16(acc/l)); }
}
__global__ void fin(const float* a, const float* x1, const float* h2, const float* g, bf16* out){
  int i=blockIdx.x*256+threadIdx.x; if(i<S_*D_){ float gg=g[i/D_]; out[i]=__float2bfloat16(gg*a[i]+(1.f-gg)*(x1[i]+h2[i])); }
}
extern "C" void kernel_launch(void* const* d_in, const int* in_sizes, int n_in, void* d_out, int out_size, void* d_ws, size_t ws_size, hipStream_t stream){
  const bf16* x=(const bf16*)d_in[0]; const bf16* wqkv=(const bf16*)d_in[1]; const bf16* wo=(const bf16*)d_in[2];
  const bf16* w1=(const bf16*)d_in[4]; const bf16* w2=(const bf16*)d_in[5]; const bf16* w3=(const bf16*)d_in[6]; const bf16* w4=(const bf16*)d_in[7]; const bf16* gw=(const bf16*)d_in[8];
  float* ws=(float*)d_ws; size_t SD=(size_t)S_*D_;
  float* xf=ws; float* lnx=xf+SD; float* g=lnx+SD; float* qkv=g+4096; float* o=qkv+3*SD; float* a=o+SD; float* h=a+SD; float* hs=h+SD; float* hw=hs+SD; float* x1=hw+SD; float* ln1=x1+SD; float* h3=ln1+SD; float* h2=h3+4*SD;
  prep<<<S_,64,0,stream>>>(x,gw,xf,lnx,g);
  gemm_naive<<<dim3(21,S_),64,0,stream>>>(xf,wqkv,qkv,S_,1344,D_,0,1);
  attn<<<dim3(S_,16),256,0,stream>>>(qkv,o);
  gemm_naive<<<dim3(7,S_),64,0,stream>>>(o,wo,a,S_,D_,D_,0,0);
  gemm_naive<<<dim3(7,S_),64,0,stream>>>(lnx,w1,h,S_,D_,D_,1,1);
  shiftk<<<S_,64,0,stream>>>(h,hs);
  gemm_naive<<<dim3(7,S_),64,0,stream>>>(hs,w2,hw,S_,D_,D_,0,0);
  addk<<<(SD+255)/256,256,0,stream>>>(xf,hw,x1,(int)SD);
  ln<<<S_,64,0,stream>>>(x1,ln1);
  gemm_naive<<<dim3(28,S_),64,0,stream>>>(ln1,w3,h3,S_,F_,D_,1,1);
  gemm_naive<<<dim3(7,S_),64,0,stream>>>(h3,w4,h2,S_,D_,F_,0,0);
  fin<<<(SD+255)/256,256,0,stream>>>(a,x1,h2,g,(bf16*)d_out);
}
```

```cpp
#include <hip/hip_runtime.h>
#include <hip/hip_bf16.h>

typedef __bf16 bf16;
typedef __bf16 bf16x2 __attribute__((ext_vector_type(2)));
typedef __bf16 bf16x4 __attribute__((ext_vector_type(4)));
typedef __bf16 bf16x8 __attribute__((ext_vector_type(8)));
typedef float f32x2 __attribute__((ext_vector_type(2)));
typedef float f32x4 __attribute__((ext_vector_type(4)));
typedef unsigned u32x2 __attribute__((ext_vector_type(2)));
typedef unsigned u32x4 __attribute__((ext_vector_type(4)));
#define LDS_AS __attribute__((address_space(3)))
#define GLB_AS __attribute__((address_space(1)))
#define DI __device__ __forceinline__

constexpr int S_ = 2048, D_ = 448, F_ = 1792;
constexpr size_t OFF_Q  = 0;
constexpr size_t OFF_K  = OFF_Q + 2097152;
constexpr size_t OFF_V  = OFF_K + 2097152;
constexpr size_t OFF_HS = OFF_V + 2097152;
constexpr size_t OFF_X1 = OFF_HS + 1835008;
constexpr size_t OFF_AC = OFF_X1 + 1835008;
constexpr size_t OFF_ST = OFF_AC + 9437184;
constexpr size_t OFF_G  = OFF_ST + 262144;
constexpr size_t OFF_CS = OFF_G + 16384;

#define WAIT_V(n) asm volatile("s_waitcnt vmcnt(" #n ")" ::: "memory")
#define BAR() do { asm volatile("s_waitcnt lgkmcnt(0)" ::: "memory"); __builtin_amdgcn_s_barrier(); asm volatile("" ::: "memory"); } while (0)
#define MFMA16(a, b, c) __builtin_amdgcn_mfma_f32_16x16x32_bf16((a), (b), (c), 0, 0, 0)

template <int IMM = 0>
DI void glds16(const void* sbase, unsigned voff, unsigned lds) {
  asm volatile("s_mov_b32 m0, %2\n\ts_nop 0\n\tglobal_load_lds_dwordx4 %0, %1 offset:%3" ::"v"(voff), "s"(sbase), "s"(lds), "i"(IMM) : "memory");
}
DI unsigned lds_addr(const char* p) { return (unsigned)(size_t)(const LDS_AS char*)p; }
DI bf16x8 ld_frag(const char* lds) { return *(const bf16x8*)lds; }
DI bf16x8 ld_tr2(const char* p0, const char* p1) {
  bf16x4 a = __builtin_amdgcn_ds_read_tr16_b64_v4bf16((LDS_AS bf16x4*)p0);
  bf16x4 b = __builtin_amdgcn_ds_read_tr16_b64_v4bf16((LDS_AS bf16x4*)p1);
  return __builtin_shufflevector(a, b, 0, 1, 2, 3, 4, 5, 6, 7);
}
DI unsigned pack2(float a, float b) {
  f32x2 t = {a, b};
  return __builtin_bit_cast(unsigned, __builtin_convertvector(t, bf16x2));
}
DI float bf_lo(unsigned u) { return __builtin_bit_cast(float, u << 16); }
DI float bf_hi(unsigned u) { return __builtin_bit_cast(float, u & 0xffff0000u); }

DI float gelu_ln(float z, float k1, float k3, float inv) {
  float t = z * z;
  float w = __builtin_fmaf(t, k3, k1);
  float e = __builtin_amdgcn_exp2f(z * w);
  float d = __builtin_fmaf(e, inv, inv);
  return z * __builtin_amdgcn_rcpf(d);
}
constexpr float GC1 = -2.0f * 0.7978845608028654f * 1.4426950408889634f;
constexpr float GC3 = GC1 * 0.044715f;

DI int swz(int row) { return ((row >> 1) & 1) | ((((row >> 2) ^ (row >> 3)) & 1) << 1); }
DI int src_chunk(int row, int lane) { return ((((lane & 7) >> 1) ^ swz(row)) << 1) | (lane & 1); }

template <int MODE, bool STATS>
DI void gemm128_loop(char* smem, const char* baseA, unsigned offA, const char* baseB0, unsigned offB0, int ldB0,
                     const char* baseB1, unsigned offB1, int ldB1, int w, int lane, f32x4 (&acc)[2][4], f32x4& cs,
                     float& sum, float& sq) {
  const int wm = w & 3, wn = w >> 2, g = lane >> 4, q = (lane >> 2) & 3, p = lane & 3, r16 = lane & 15;
  const unsigned l0 = lds_addr(smem);
  auto issue = [&](int st, int slot) {
    unsigned base = l0 + slot * 32768;
    if (MODE == 0) {
      glds16(baseA + st * 128, offA, base + w * 2048);
      glds16(baseA + st * 128 + 8 * D_ * 2, offA, base + w * 2048 + 1024);
    } else {
      glds16(baseA + st * 2048, offA, base + w * 2048);
      glds16<1024>(baseA + st * 2048, offA, base + w * 2048);
    }
    glds16(baseB0 + (size_t)st * 64 * ldB0, offB0, base + 16384 + w * 1024);
    glds16(baseB1 + (size_t)st * 64 * ldB1, offB1, base + 24576 + w * 1024);
  };
  int aoff[2][2];
#pragma unroll
  for (int rf = 0; rf < 2; ++rf)
#pragma unroll
    for (int u = 0; u < 2; ++u) {
      if (MODE == 0) {
        int row = 32 * wm + 16 * rf + r16;
        aoff[rf][u] = row * 128 + (((4 * u + g) ^ (row & 7)) << 4);
      } else {
        aoff[rf][u] = ((2 * wm + rf) * 2 + u) * 1024 + lane * 16;
      }
    }
  int boff[2][4];
#pragma unroll
  for (int t = 0; t < 2; ++t)
#pragma unroll
    for (int j = 0; j < 4; ++j) {
      int row = (MODE == 0) ? (8 * g + 4 * t + q) : (16 * t + 4 * g + q);
      boff[t][j] = 16384 + wn * 8192 + row * 128 + ((j ^ swz(row)) << 5) + p * 8;
    }
  bf16x8 ones;
#pragma unroll
  for (int i = 0; i < 8; ++i) ones[i] = (bf16)1.0f;
  bf16x2 one2 = {(bf16)1.0f, (bf16)1.0f};

  auto compute = [&](int slot) {
    const char* base = smem + slot * 32768;
#pragma unroll
    for (int u = 0; u < 2; ++u) {
      bf16x8 a0 = ld_frag(base + aoff[0][u]);
      bf16x8 a1 = ld_frag(base + aoff[1][u]);
      bf16x8 bw;
#pragma unroll
      for (int j = 0; j < 4; ++j) {
        bf16x8 b = ld_tr2(base + u * 4096 + boff[0][j], base + u * 4096 + boff[1][j]);
        acc[0][j] = MFMA16(b, a0, acc[0][j]);
        acc[1][j] = MFMA16(b, a1, acc[1][j]);
      }
      if (STATS) {
        int jw = wm << 5;
        bw = ld_tr2(base + u * 4096 + (boff[0][0] ^ jw), base + u * 4096 + (boff[1][0] ^ jw));
        cs = MFMA16(bw, ones, cs);
        bf16x8 as = wn ? a1 : a0;
#pragma unroll
        for (int i = 0; i < 4; ++i) {
          bf16x2 v = {as[2 * i], as[2 * i + 1]};
          sum = __builtin_amdgcn_fdot2_f32_bf16(v, one2, sum, false);
          sq = __builtin_amdgcn_fdot2_f32_bf16(v, v, sq, false);
        }
      }
    }
  };
  issue(0, 0); issue(1, 1); issue(2, 2); issue(3, 3); issue(4, 4);
  WAIT_V(16); BAR(); compute(0);
  WAIT_V(12); BAR(); issue(5, 0); compute(1);
  WAIT_V(12); BAR(); issue(6, 1); compute(2);
  WAIT_V(12); BAR(); compute(3);
  WAIT_V(8); BAR(); compute(4);
  WAIT_V(4); BAR(); compute(0);
  WAIT_V(0); BAR(); compute(1);
}

__global__ __launch_bounds__(512, 2) void k1_qkv_h(const bf16* __restrict__ x, const bf16* __restrict__ wqkv,
                                                   const bf16* __restrict__ w1, const bf16* __restrict__ w3,
                                                   const bf16* __restrict__ gw, char* __restrict__ ws) {
  __shared__ __attribute__((aligned(16))) char smem[163840];
  const int tid = threadIdx.x, lane = tid & 63;
  const int w = __builtin_amdgcn_readfirstlane(tid >> 6);
  const int xcd = blockIdx.x & 7, idx = blockIdx.x >> 3;
  if (idx >= 28) {
    const int hb = xcd * 4 + (idx - 28);
    {
      bf16x8 gv = {};
      if (lane < 56) gv = *(const bf16x8*)(gw + lane * 8);
      float* G = (float*)(ws + OFF_G);
#pragma unroll
      for (int r = 0; r < 8; ++r) {
        int row = hb * 64 + w * 8 + r;
        bf16x8 xv = {};
        if (lane < 56) xv = *(const bf16x8*)(x + row * D_ + lane * 8);
        float s = 0.f;
#pragma unroll
        for (int i = 0; i < 4; ++i) {
          bf16x2 a = {xv[2 * i], xv[2 * i + 1]}, b = {gv[2 * i], gv[2 * i + 1]};
          s = __builtin_amdgcn_fdot2_f32_bf16(a, b, s, false);
        }
#pragma unroll
        for (int o = 32; o; o >>= 1) s += __shfl_xor(s, o);
        if (lane == 0) {
          f32x2 gg = {1.f / (1.f + __expf(-s)), 1.f / (1.f + __expf(s))};
          *(f32x2*)(G + row * 2) = gg;
        }
      }
    }
    {
      float* red = (float*)smem;
      if (tid < 448) {
        int ch = tid % 7, rg = tid / 7;
        float a[8] = {};
#pragma unroll
        for (int r = 0; r < 7; ++r) {
          bf16x8 v = *(const bf16x8*)(w3 + (size_t)(rg * 7 + r) * F_ + hb * 56 + ch * 8);
#pragma unroll
          for (int i = 0; i < 8; ++i) a[i] += (float)v[i];
        }
#pragma unroll
        for (int i = 0; i < 8; ++i) red[rg * 56 + ch * 8 + i] = a[i];
      }
      __syncthreads();
      if (tid < 56) {
        float s = 0.f;
        for (int r = 0; r < 64; ++r) s += red[r * 56 + tid];
        ((float*)(ws + OFF_CS))[hb * 56 + tid] = s;
      }
    }
    return;
  }
  const int panel = 2 * xcd + (idx & 1), ct = idx >> 1;
  const int wm = w & 3, wn = w >> 2, g = lane >> 4, r16 = lane & 15;
  const char* baseA = (const char*)(x + (size_t)panel * 128 * D_);
  const unsigned offA = ((16 * w + (lane >> 3)) * D_ + (((lane & 7) ^ (lane >> 3)) << 3)) * 2;
  const int brow = 8 * w + (lane >> 3), bch = src_chunk(brow, lane);
  const int u0 = 2 * ct, u1 = 2 * ct + 1;
  const int ld0 = u0 < 21 ? 2688 : 896, ld1 = u1 < 21 ? 2688 : 896;
  const char* baseB0 = u0 < 21 ? (const char*)(wqkv + u0 * 64) : (const char*)(w1 + (u0 - 21) * 64);
  const char* baseB1 = u1 < 21 ? (const char*)(wqkv + u1 * 64) : (const char*)(w1 + (u1 - 21) * 64);
  const unsigned offB0 = brow * ld0 + bch * 16, offB1 = brow * ld1 + bch * 16;
  f32x4 acc[2][4] = {};
  f32x4 cs = {};
  float sum = 0.f, sq = 0.f;
  const int u = 2 * ct + wn;
  const int type = u / 7;
  const int nb = (u - type * 7) * 64;
  if (ct < 10) {
    gemm128_loop<0, false>(smem, baseA, offA, baseB0, offB0, ld0, baseB1, offB1, ld1, w, lane, acc, cs, sum, sq);
  } else {
    gemm128_loop<0, true>(smem, baseA, offA, baseB0, offB0, ld0, baseB1, offB1, ld1, w, lane, acc, cs, sum, sq);
    float* ex = (float*)(smem + 3 * 32768);
    sum += __shfl_xor(sum, 16); sum += __shfl_xor(sum, 32);
    sq += __shfl_xor(sq, 16); sq += __shfl_xor(sq, 32);
    if (r16 == 0) *(f32x4*)(ex + (4 * wn + wm) * 16 + 4 * g) = cs;
    if (g == 0) { f32x2 t = {sum, sq}; *(f32x2*)(ex + 128 + (32 * wm + 16 * wn + r16) * 2) = t; }
    BAR();
  }
  if (type < 3) {
#pragma unroll
    for (int j = 0; j < 4; ++j) {
      int n = nb + 16 * j + 4 * g;
      int head = n / 28, G = (n - head * 28) >> 2;
#pragma unroll
      for (int rf = 0; rf < 2; ++rf) {
        int tok = panel * 128 + 32 * wm + 16 * rf + r16;
        u32x2 v = {pack2(acc[rf][j][0], acc[rf][j][1]), pack2(acc[rf][j][2], acc[rf][j][3])};
        char* dst;
        if (type < 2) dst = ws + OFF_Q + (size_t)type * 2097152 + ((size_t)((head * 128 + (tok >> 4)) * 64 + (G >> 1) * 16 + r16) << 4) + (G & 1) * 8;
        else dst = ws + OFF_V + ((size_t)(head * 2048 + tok) << 6) + ((((G >> 2) ^ (tok >> 2)) & 1) << 5) + (G & 3) * 8;
        *(u32x2*)dst = v;
        if (G == 6) {
          u32x2 pad = {type == 2 ? 0x3f80u : 0u, 0u};
          *(u32x2*)(dst + 8) = pad;
        }
      }
    }
  } else {
    const float* ex = (const float*)(smem + 3 * 32768);
#pragma unroll
    for (int rf = 0; rf < 2; ++rf) {
      f32x2 st = *(const f32x2*)(ex + 128 + (32 * wm + 16 * rf + r16) * 2);
      float mean = st[0] * (1.f / D_);
      float var = fmaxf(st[1] * (1.f / D_) - mean * mean, 0.f);
      float a = __builtin_amdgcn_rsqf(var + 1e-5f);
      float k1 = GC1 * a, k3 = GC3 * a * a * a, inv = __builtin_amdgcn_rcpf(a);
      int tok = panel * 128 + 32 * wm + 16 * rf + r16;
#pragma unroll
      for (int j = 0; j < 4; ++j) {
        f32x4 c4 = *(const f32x4*)(ex + (4 * wn + j) * 16 + 4 * g);
        float y[4];
#pragma unroll
        for (int i = 0; i < 4; ++i) y[i] = gelu_ln(__builtin_fmaf(-mean, c4[i], acc[rf][j][i]), k1, k3, inv);
        int n = nb + 16 * j;
        int grp = n / 112;
        int sh = grp == 0 ? 1 : grp == 1 ? -1 : grp == 2 ? 2 : -2;
        int t2 = tok + sh;
        bool ok = (unsigned)t2 < (unsigned)S_;
        t2 &= S_ - 1;
        u32x2 v = {ok ? pack2(y[0], y[1]) : 0u, ok ? pack2(y[2], y[3]) : 0u};
        char* dst = ws + OFF_HS + ((size_t)(((t2 >> 4) * 14 + (n >> 5)) * 64 + g * 16 + (t2 & 15)) << 4) + ((n >> 4) & 1) * 8;
        *(u32x2*)dst = v;
      }
    }
  }
}

__global__ __launch_bounds__(512, 2) void k2_attn_x1(const bf16* __restrict__ x, const bf16* __restrict__ w2,
                                                     char* __restrict__ ws) {
  __shared__ __attribute__((aligned(16))) char smem[163840];
  const int tid = threadIdx.x, lane = tid & 63;
  const int w = __builtin_amdgcn_readfirstlane(tid >> 6);
  const int xcd = blockIdx.x & 7, idx = blockIdx.x >> 3;
  const int g = lane >> 4, q = (lane >> 2) & 3, p = lane & 3, r16 = lane & 15;
  const int qb = 2 * xcd + (idx & 1), head = idx >> 1;
  const int q0 = qb * 128;
  char* sK = smem;
  char* sV = smem + 24576;
  char* sA = smem + 49152;
  char* sB = sA + 57344;
  const unsigned l0 = lds_addr(smem);
  const bool has_gemm = idx < 28;
  const int rt64 = 4 * xcd + (idx & 3), ct = idx >> 2;
  const int kh = w >> 2, mh = (w >> 1) & 1, nh = w & 1;

  bf16x8 qf;
  {
    const char* qsrc = ws + OFF_Q + ((size_t)((head * 128 + qb * 8 + w) * 64 + lane) << 4);
    asm volatile("global_load_dwordx4 %0, %1, off" : "=v"(qf) : "v"(qsrc) : "memory");
  }
#pragma unroll
  for (int m = 0; m < 3; ++m) {
    int lt = w + 8 * m;
    int kt = qb * 8 - 16 + lt;
    kt = kt < 0 ? 0 : kt;
    unsigned off = (unsigned)((head * 128 + kt) * 64 + lane) << 4;
    glds16(ws + OFF_K, off, l0 + lt * 1024);
    glds16(ws + OFF_V, off, l0 + 24576 + lt * 1024);
  }
  if (has_gemm) {
    const unsigned offA = (unsigned)(((rt64 * 4 + (w >> 1)) * 14 + 7 * (w & 1)) * 64 + lane) << 4;
    const int brow = 8 * w + (lane >> 3);
    const unsigned offB = (brow * D_ + ct * 64 + src_chunk(brow, lane) * 8) * 2;
#pragma unroll
    for (int m = 0; m < 7; ++m) {
      glds16(ws + OFF_HS + m * 1024, offA, l0 + 49152 + ((w >> 1) * 14 + 7 * (w & 1) + m) * 1024);
      glds16((const char*)w2 + (size_t)m * 64 * D_ * 2, offB, l0 + 49152 + 57344 + (w + 8 * m) * 1024);
    }
    WAIT_V(14);
  } else {
    WAIT_V(0);
  }
  asm volatile("" : "+v"(qf)::"memory");
  BAR();
  const int qw = q0 + 16 * w;
  f32x4 s[17];
#pragma unroll
  for (int t = 0; t < 17; ++t) {
    bf16x8 kf = ld_frag(sK + (w + t) * 1024 + lane * 16);
    f32x4 z = {};
    s[t] = MFMA16(kf, qf, z);
  }
  const float NEG = -__builtin_inff();
#pragma unroll
  for (int i = 0; i < 4; ++i) {
    int b = 4 * g + i;
    s[0][i] = (b > r16) ? s[0][i] : NEG;
    s[16][i] = (b <= r16) ? s[16][i] : NEG;
  }
  if (qw < 256) {
    int tstart = 16 - (qw >> 4);
#pragma unroll
    for (int t = 0; t < 16; ++t)
      if (t < tstart) s[t] = f32x4{NEG, NEG, NEG, NEG};
  }
  float mx = NEG;
#pragma unroll
  for (int t = 0; t < 17; ++t) mx = fmaxf(fmaxf(mx, fmaxf(s[t][0], s[t][1])), fmaxf(s[t][2], s[t][3]));
  mx = fmaxf(mx, __shfl_xor(mx, 16));
  mx = fmaxf(mx, __shfl_xor(mx, 32));
  constexpr float SC = 0.1889822365046136f * 1.4426950408889634f;
  const float mc = -mx * SC;
  f32x4 o[2] = {};
  const int vrow = 4 * g + q;
  const int voff0 = vrow * 64 + ((g & 1) << 5) + p * 8;
  const int voff1 = voff0 ^ 32;
#pragma unroll
  for (int kk = 0; kk < 9; ++kk) {
    u32x4 pk;
    pk[0] = pack2(__builtin_amdgcn_exp2f(__builtin_fmaf(s[2 * kk][0], SC, mc)), __builtin_amdgcn_exp2f(__builtin_fmaf(s[2 * kk][1], SC, mc)));
    pk[1] = pack2(__builtin_amdgcn_exp2f(__builtin_fmaf(s[2 * kk][2], SC, mc)), __builtin_amdgcn_exp2f(__builtin_fmaf(s[2 * kk][3], SC, mc)));
    if (kk < 8) {
      pk[2] = pack2(__builtin_amdgcn_exp2f(__builtin_fmaf(s[2 * kk + 1][0], SC, mc)), __builtin_amdgcn_exp2f(__builtin_fmaf(s[2 * kk + 1][1], SC, mc)));
      pk[3] = pack2(__builtin_amdgcn_exp2f(__builtin_fmaf(s[2 * kk + 1][2], SC, mc)), __builtin_amdgcn_exp2f(__builtin_fmaf(s[2 * kk + 1][3], SC, mc)));
    } else {
      pk[2] = 0u; pk[3] = 0u;
    }
    bf16x8 pf = __builtin_bit_cast(bf16x8, pk);
    const char* vb = sV + (w + 2 * kk) * 1024;
    const int second = kk < 8 ? 1024 : 0;
    bf16x8 v0 = ld_tr2(vb + voff0, vb + second + voff0);
    bf16x8 v1 = ld_tr2(vb + voff1, vb + second + voff1);
    o[0] = MFMA16(v0, pf, o[0]);
    o[1] = MFMA16(v1, pf, o[1]);
  }
  {
    float l = __shfl(o[1][0], 48 + r16);
    float gt = ((const float*)(ws + OFF_G))[(qw + r16) * 2];
    float sc = gt * __builtin_amdgcn_rcpf(l);
    u32x4 v;
    v[0] = pack2(o[0][0] * sc, o[0][1] * sc);
    v[1] = pack2(o[0][2] * sc, o[0][3] * sc);
    v[2] = g == 3 ? 0u : pack2(o[1][0] * sc, o[1][1] * sc);
    v[3] = g == 3 ? 0u : pack2(o[1][2] * sc, o[1][3] * sc);
    *(u32x4*)(ws + OFF_AC + ((size_t)(((qw >> 4) * 72 + head) * 64 + lane) << 4)) = v;
  }
  if (!has_gemm) return;
  const int tok = rt64 * 64 + 32 * mh + 16 * kh + r16;
  u32x2 xr[2];
#pragma unroll
  for (int j = 0; j < 2; ++j) xr[j] = *(const u32x2*)(x + (size_t)tok * D_ + ct * 64 + 32 * nh + 16 * j + 4 * g);
  WAIT_V(0); BAR();
  f32x4 acc[2][2] = {};
  {
    const int f4 = swz(4 * g + q);
    const int brow = (4 * g + q) * 128 + p * 8;
#pragma unroll
    for (int m = 0; m < 7; ++m) {
      int ks = 2 * m + kh;
      bf16x8 a0 = ld_frag(sA + ((2 * mh) * 14 + ks) * 1024 + lane * 16);
      bf16x8 a1 = ld_frag(sA + ((2 * mh + 1) * 14 + ks) * 1024 + lane * 16);
#pragma unroll
      for (int j = 0; j < 2; ++j) {
        const char* bp = sB + ks * 4096 + brow + (((2 * nh + j) ^ f4) << 5);
        bf16x8 b = ld_tr2(bp, bp + 2048);
        acc[0][j] = MFMA16(b, a0, acc[0][j]);
        acc[1][j] = MFMA16(b, a1, acc[1][j]);
      }
    }
  }
  {
    f32x4* ex = (f32x4*)smem;
#pragma unroll
    for (int j = 0; j < 2; ++j) ex[(w * 2 + j) * 64 + lane] = kh ? acc[0][j] : acc[1][j];
    BAR();
    float sum = 0.f, sq = 0.f;
    u32x4 v;
#pragma unroll
    for (int j = 0; j < 2; ++j) {
      f32x4 r = ex[((w ^ 4) * 2 + j) * 64 + lane] + (kh ? acc[1][j] : acc[0][j]);
      unsigned lo = pack2(r[0] + bf_lo(xr[j][0]), r[1] + bf_hi(xr[j][0]));
      unsigned hi = pack2(r[2] + bf_lo(xr[j][1]), r[3] + bf_hi(xr[j][1]));
      v[2 * j] = lo; v[2 * j + 1] = hi;
      float e0 = bf_lo(lo), e1 = bf_hi(lo), e2 = bf_lo(hi), e3 = bf_hi(hi);
      sum += (e0 + e1) + (e2 + e3);
      sq += (e0 * e0 + e1 * e1) + (e2 * e2 + e3 * e3);
    }
    *(u32x4*)(ws + OFF_X1 + ((size_t)(((tok >> 4) * 14 + 2 * ct + nh) * 64 + lane) << 4)) = v;
    sum += __shfl_xor(sum, 16); sum += __shfl_xor(sum, 32);
    sq += __shfl_xor(sq, 16); sq += __shfl_xor(sq, 32);
    if (g == 0) { f32x2 t = {sum, sq}; *(f32x2*)(ws + OFF_ST + (size_t)tok * 128 + (2 * ct + nh) * 8) = t; }
  }
}

__global__ __launch_bounds__(512, 2) void k3_h3(const bf16* __restrict__ w3, char* __restrict__ ws) {
  __shared__ __attribute__((aligned(16))) char smem[163840];
  const int tid = threadIdx.x, lane = tid & 63;
  const int w = __builtin_amdgcn_readfirstlane(tid >> 6);
  const int xcd = blockIdx.x & 7, idx = blockIdx.x >> 3;
  const int panel = 2 * xcd + (idx & 1), ct = idx >> 1;
  const int wm = w & 3, wn = w >> 2, g = lane >> 4, r16 = lane & 15;
  f32x4 st[2][2], c4[4];
  float g1[2];
#pragma unroll
  for (int rf = 0; rf < 2; ++rf) {
    int tok = panel * 128 + 32 * wm + 16 * rf + r16;
    st[rf][0] = *(const f32x4*)(ws + OFF_ST + (size_t)tok * 128 + g * 32);
    st[rf][1] = *(const f32x4*)(ws + OFF_ST + (size_t)tok * 128 + g * 32 + 16);
    g1[rf] = ((const float*)(ws + OFF_G))[tok * 2 + 1];
  }
#pragma unroll
  for (int j = 0; j < 4; ++j) c4[j] = *(const f32x4*)(ws + OFF_CS + (size_t)(ct * 128 + wn * 64 + 16 * j + 4 * g) * 4);
  asm volatile("" ::: "memory");
  const char* baseA = ws + OFF_X1 + (size_t)panel * 8 * 14 * 1024;
  const unsigned offA = (unsigned)(w * 14 * 64 + lane) << 4;
  const int brow = 8 * w + (lane >> 3);
  const unsigned offB = brow * F_ * 2 + src_chunk(brow, lane) * 16;
  const char* baseB = (const char*)(w3 + ct * 128);
  f32x4 acc[2][4] = {};
  f32x4 cs = {};
  float sum = 0.f, sq = 0.f;
  gemm128_loop<1, false>(smem, baseA, offA, baseB, offB, F_ * 2, baseB + 128, offB, F_ * 2, w, lane, acc, cs, sum, sq);
#pragma unroll
  for (int rf = 0; rf < 2; ++rf) {
    float s1 = st[rf][0][0] + st[rf][0][2], s2 = st[rf][0][1] + st[rf][0][3];
    if (g < 3) { s1 += st[rf][1][0] + st[rf][1][2]; s2 += st[rf][1][1] + st[rf][1][3]; }
    s1 += __shfl_xor(s1, 16); s1 += __shfl_xor(s1, 32);
    s2 += __shfl_xor(s2, 16); s2 += __shfl_xor(s2, 32);
    float mean = s1 * (1.f / D_);
    float var = fmaxf(s2 * (1.f / D_) - mean * mean, 0.f);
    float a = __builtin_amdgcn_rsqf(var + 1e-5f);
    float k1 = GC1 * a, k3 = GC3 * a * a * a, inv = __builtin_amdgcn_rcpf(fmaxf(a * g1[rf], 1e-30f));
    int tt = panel * 8 + 2 * wm + rf;
#pragma unroll
    for (int jj = 0; jj < 2; ++jj) {
      u32x4 v;
#pragma unroll
      for (int h = 0; h < 2; ++h) {
        int j = 2 * jj + h;
        float y[4];
#pragma unroll
        for (int i = 0; i < 4; ++i) y[i] = gelu_ln(__builtin_fmaf(-mean, c4[j][i], acc[rf][j][i]), k1, k3, inv);
        v[2 * h] = pack2(y[0], y[1]); v[2 * h + 1] = pack2(y[2], y[3]);
      }
      int ks = 16 + ct * 4 + wn * 2 + jj;
      *(u32x4*)(ws + OFF_AC + ((size_t)((tt * 72 + ks) * 64 + lane) << 4)) = v;
    }
  }
}

__global__ __launch_bounds__(512, 2) void k4_out(const bf16* __restrict__ wo, const bf16* __restrict__ w4,
                                                 const char* __restrict__ ws, bf16* __restrict__ out) {
  __shared__ __attribute__((aligned(16))) char smem[163840];
  const int tid = threadIdx.x, lane = tid & 63;
  const int w = __builtin_amdgcn_readfirstlane(tid >> 6);
  const int xcd = blockIdx.x & 7, idx = blockIdx.x >> 3;
  const int rt64 = 4 * xcd + (idx & 3), ct = idx >> 2;
  const int kh = w >> 2, mh = (w >> 1) & 1, nh = w & 1;
  const int g = lane >> 4, q = (lane >> 2) & 3, p = lane & 3, r16 = lane & 15;
  const int tok = rt64 * 64 + 32 * mh + 16 * kh + r16;
  u32x4 x1v = *(const u32x4*)(ws + OFF_X1 + ((size_t)(((tok >> 4) * 14 + 2 * ct + nh) * 64 + lane) << 4));
  float g1 = ((const float*)(ws + OFF_G))[tok * 2 + 1];
  asm volatile("" ::: "memory");
  const unsigned l0 = lds_addr(smem);
  const char* baseA = ws + OFF_AC + (size_t)rt64 * 4 * 72 * 1024;
  const unsigned offA = (unsigned)(((w >> 1) * 72 + 2 * (w & 1)) * 64 + lane) << 4;
  const int rl = 16 * w + (lane >> 3);
  const int c0 = src_chunk(rl, lane), c1 = src_chunk(rl + 8, lane);
  const int d0 = rl & 31, d1 = d0 + 8;
  const unsigned offO0 = ((28 * (rl >> 5) + (d0 < 27 ? d0 : 27)) * D_ + c0 * 8) * 2;
  const unsigned offO1 = ((28 * (rl >> 5) + (d1 < 27 ? d1 : 27)) * D_ + c1 * 8) * 2;
  const unsigned offW0 = (rl * D_ + c0 * 8) * 2, offW1 = ((rl + 8) * D_ + c1 * 8) * 2;
  const char* baseO = (const char*)(wo + ct * 64);
  const char* baseW = (const char*)(w4 + ct * 64);
  auto issue = [&](int st, int slot) {
    unsigned base = l0 + slot * 32768;
    glds16(baseA + (size_t)st * 4096, offA, base + w * 2048);
    glds16<1024>(baseA + (size_t)st * 4096, offA, base + w * 2048);
    if (st < 4) {
      glds16(baseO + (size_t)st * 112 * D_ * 2, offO0, base + 16384 + w * 2048);
      glds16(baseO + (size_t)st * 112 * D_ * 2, offO1, base + 16384 + w * 2048 + 1024);
    } else {
      glds16(baseW + (size_t)(st - 4) * 128 * D_ * 2, offW0, base + 16384 + w * 2048);
      glds16(baseW + (size_t)(st - 4) * 128 * D_ * 2, offW1, base + 16384 + w * 2048 + 1024);
    }
  };
  f32x4 acc[2][2] = {};
  const int f4 = swz(4 * g + q);
  const int brow = 16384 + (4 * g + q) * 128 + p * 8 + kh * 8192;
  const int b0 = brow + (((2 * nh) ^ f4) << 5), b1 = brow + (((2 * nh + 1) ^ f4) << 5);
  const int a0o = ((2 * mh) * 4 + 2 * kh) * 1024 + lane * 16;
  auto compute = [&](int slot) {
    const char* base = smem + slot * 32768;
#pragma unroll
    for (int u = 0; u < 2; ++u) {
      bf16x8 a0 = ld_frag(base + a0o + u * 1024);
      bf16x8 a1 = ld_frag(base + a0o + 4096 + u * 1024);
      bf16x8 bb0 = ld_tr2(base + b0 + u * 4096, base + b0 + u * 4096 + 2048);
      bf16x8 bb1 = ld_tr2(base + b1 + u * 4096, base + b1 + u * 4096 + 2048);
      acc[0][0] = MFMA16(bb0, a0, acc[0][0]);
      acc[1][0] = MFMA16(bb0, a1, acc[1][0]);
      acc[0][1] = MFMA16(bb1, a0, acc[0][1]);
      acc[1][1] = MFMA16(bb1, a1, acc[1][1]);
    }
  };
  issue(0, 0); issue(1, 1); issue(2, 2); issue(3, 3); issue(4, 4);
  WAIT_V(16); BAR(); compute(0);
  int slot = 1, fill = 0;
#pragma unroll 1
  for (int st = 1; st < 14; ++st) {
    WAIT_V(12); BAR();
    issue(st + 4, fill);
    compute(slot);
    fill = slot;
    slot = slot == 4 ? 0 : slot + 1;
  }
  WAIT_V(12); BAR(); compute(4);
  WAIT_V(8); BAR(); compute(0);
  WAIT_V(4); BAR(); compute(1);
  WAIT_V(0); BAR(); compute(2);
  {
    f32x4* ex = (f32x4*)(smem + 3 * 32768);
#pragma unroll
    for (int j = 0; j < 2; ++j) ex[(w * 2 + j) * 64 + lane] = kh ? acc[0][j] : acc[1][j];
    BAR();
#pragma unroll
    for (int j = 0; j < 2; ++j) {
      f32x4 r = ex[((w ^ 4) * 2 + j) * 64 + lane] + (kh ? acc[1][j] : acc[0][j]);
      u32x2 v;
      v[0] = pack2(__builtin_fmaf(g1, bf_lo(x1v[2 * j]), r[0]), __builtin_fmaf(g1, bf_hi(x1v[2 * j]), r[1]));
      v[1] = pack2(__builtin_fmaf(g1, bf_lo(x1v[2 * j + 1]), r[2]), __builtin_fmaf(g1, bf_hi(x1v[2 * j + 1]), r[3]));
      *(u32x2*)(out + (size_t)tok * D_ + ct * 64 + 32 * nh + 16 * j + 4 * g) = v;
    }
  }
}

extern "C" void kernel_launch(void* const* d_in, const int* in_sizes, int n_in, void* d_out, int out_size, void* d_ws,
                              size_t ws_size, hipStream_t stream) {
  const bf16* x = (const bf16*)d_in[0];
  const bf16* wqkv = (const bf16*)d_in[1];
  const bf16* wo = (const bf16*)d_in[2];
  const bf16* w1 = (const bf16*)d_in[4];
  const bf16* w2 = (const bf16*)d_in[5];
  const bf16* w3 = (const bf16*)d_in[6];
  const bf16* w4 = (const bf16*)d_in[7];
  const bf16* gw = (const bf16*)d_in[8];
  char* ws = (char*)d_ws;
  k1_qkv_h<<<256, 512, 0, stream>>>(x, wqkv, w1, w3, gw, ws);
  k2_attn_x1<<<256, 512, 0, stream>>>(x, w2, ws);
  k3_h3<<<224, 512, 0, stream>>>(w3, ws);
  k4_out<<<224, 512, 0, stream>>>(wo, w4, ws, (bf16*)d_out);
}
```

```cpp
#include <hip/hip_runtime.h>
#include <hip/hip_bf16.h>

typedef __bf16 bf16;
typedef __bf16 bf16x2 __attribute__((ext_vector_type(2)));
typedef __bf16 bf16x4 __attribute__((ext_vector_type(4)));
typedef __bf16 bf16x8 __attribute__((ext_vector_type(8)));
typedef float f32x2 __attribute__((ext_vector_type(2)));
typedef float f32x4 __attribute__((ext_vector_type(4)));
typedef unsigned u32x2 __attribute__((ext_vector_type(2)));
typedef unsigned u32x4 __attribute__((ext_vector_type(4)));
#define LDS_AS __attribute__((address_space(3)))
#define DI __device__ __forceinline__

constexpr int S_ = 2048, D_ = 448, F_ = 1792;
constexpr size_t OFF_QKV = 0;
constexpr size_t OFF_HS = OFF_QKV + 5505024;
constexpr size_t OFF_X1 = OFF_HS + 1835008;
constexpr size_t OFF_AC = OFF_X1 + 1835008;
constexpr size_t OFF_ST = OFF_AC + 9437184;
constexpr size_t OFF_G  = OFF_ST + 262144;
constexpr size_t OFF_CS = OFF_G + 16384;
constexpr size_t OFF_FL = OFF_CS + 8192;
constexpr size_t WS_USED = OFF_FL + 3 * 256 * 128;

#define STAMP_DECL
#define STAMP(i)
#define STAMP_FLUSH(ws, kid)
#define STAMP_ARG
#define STAMP_PASS

#define WAIT_V(n) asm volatile("s_waitcnt vmcnt(" #n ")" ::: "memory")
#define BAR() do { asm volatile("s_waitcnt lgkmcnt(0)" ::: "memory"); __builtin_amdgcn_s_barrier(); asm volatile("" ::: "memory"); } while (0)
#define MFMA16(a, b, c) __builtin_amdgcn_mfma_f32_16x16x32_bf16((a), (b), (c), 0, 0, 0)

template <int IMM = 0>
DI void glds16(const void* sbase, unsigned voff, unsigned lds) {
  asm volatile("s_mov_b32 m0, %2\n\ts_nop 0\n\tglobal_load_lds_dwordx4 %0, %1 offset:%3" ::"v"(voff), "s"(sbase), "s"(lds), "i"(IMM) : "memory");
}
template <int LOFF>
DI void glds16c(const void* sbase, unsigned voff, unsigned ldsbase) {
  asm volatile("s_add_u32 m0, %2, %3\n\ts_nop 0\n\tglobal_load_lds_dwordx4 %0, %1" ::"v"(voff), "s"(sbase), "s"(ldsbase), "i"(LOFF) : "memory", "scc");
}
DI void touch8k(unsigned& sink, const void* sbase, unsigned chunk, int lane) {
  asm volatile("global_load_dword %0, %1, %2" : "+v"(sink) : "v"(chunk * 8192u + lane * 128u), "s"(sbase) : "memory");
}
DI void touch_keep(unsigned& sink) { asm volatile("" : "+v"(sink)::"memory"); }
DI unsigned lds_addr(const char* p) { return (unsigned)(size_t)(const LDS_AS char*)p; }
DI bf16x8 ld_frag(const char* lds) { return *(const bf16x8*)lds; }
DI bf16x8 ld_tr2(const char* p0, const char* p1) {
  bf16x4 a = __builtin_amdgcn_ds_read_tr16_b64_v4bf16((LDS_AS bf16x4*)p0);
  bf16x4 b = __builtin_amdgcn_ds_read_tr16_b64_v4bf16((LDS_AS bf16x4*)p1);
  return __builtin_shufflevector(a, b, 0, 1, 2, 3, 4, 5, 6, 7);
}
DI unsigned pack2(float a, float b) {
  f32x2 t = {a, b};
  return __builtin_bit_cast(unsigned, __builtin_convertvector(t, bf16x2));
}
DI float bf_lo(unsigned u) { return __builtin_bit_cast(float, u << 16); }
DI float bf_hi(unsigned u) { return __builtin_bit_cast(float, u & 0xffff0000u); }
DI float xsum4(float x) {
  auto a = __builtin_amdgcn_permlane16_swap(__float_as_uint(x), __float_as_uint(x), false, false);
  x = __uint_as_float(a[0]) + __uint_as_float(a[1]);
  auto b = __builtin_amdgcn_permlane32_swap(__float_as_uint(x), __float_as_uint(x), false, false);
  return __uint_as_float(b[0]) + __uint_as_float(b[1]);
}
DI float xmax4(float x) {
  auto a = __builtin_amdgcn_permlane16_swap(__float_as_uint(x), __float_as_uint(x), false, false);
  x = fmaxf(__uint_as_float(a[0]), __uint_as_float(a[1]));
  auto b = __builtin_amdgcn_permlane32_swap(__float_as_uint(x), __float_as_uint(x), false, false);
  return fmaxf(__uint_as_float(b[0]), __uint_as_float(b[1]));
}

DI float gelu_ln(float z, float k1, float k3, float inv) {
  float t = z * z;
  float w = __builtin_fmaf(t, k3, k1);
  float e = __builtin_amdgcn_exp2f(z * w);
  float d = __builtin_fmaf(e, inv, inv);
  return z * __builtin_amdgcn_rcpf(d);
}
constexpr float GC1 = -2.0f * 0.7978845608028654f * 1.4426950408889634f;
constexpr float GC3 = GC1 * 0.044715f;

DI int swz(int row) { return ((row >> 1) & 1) | ((((row >> 2) ^ (row >> 3)) & 1) << 1); }
DI int src_chunk(int row, int lane) { return ((((lane & 7) >> 1) ^ swz(row)) << 1) | (lane & 1); }

template <int N> DI void wait_v() { asm volatile("s_waitcnt vmcnt(%0)" ::"i"(N) : "memory"); }
#define SB() __builtin_amdgcn_sched_barrier(0)
#define SGB(mask, n) __builtin_amdgcn_sched_group_barrier(mask, n, 0)
DI f32x2 gelu2(f32x2 z, float k1, float k3, float inv) {
  f32x2 t = z * z;
  f32x2 w = __builtin_elementwise_fma(t, f32x2{k3, k3}, f32x2{k1, k1});
  f32x2 u = z * w;
  f32x2 e = {__builtin_amdgcn_exp2f(u[0]), __builtin_amdgcn_exp2f(u[1])};
  f32x2 d = __builtin_elementwise_fma(e, f32x2{inv, inv}, f32x2{inv, inv});
  f32x2 r = {__builtin_amdgcn_rcpf(d[0]), __builtin_amdgcn_rcpf(d[1])};
  return z * r;
}
DI unsigned pack2v(f32x2 t) { return __builtin_bit_cast(unsigned, __builtin_convertvector(t, bf16x2)); }

constexpr int K1_A = 114688;
template <bool STATS>
DI void k1_compute(char* smem, const char* xrows, int w, int lane, f32x4 (&acc)[2][8], f32x4& cs, f32x4 (&sum)[2],
                   f32x4 (&sq)[2] STAMP_ARG) {
  const int g = lane >> 4, q = (lane >> 2) & 3, p = lane & 3, r16 = lane & 15;
  const char* sa = smem + K1_A + w * 12288;
  const unsigned la = lds_addr(smem) + K1_A + w * 12288;
  const unsigned xoff = ((32 * w + (lane >> 3)) * D_ + (((lane & 7) ^ (lane >> 3)) << 3)) * 2;
  auto issueA = [&](int st, int slot) {
#pragma unroll
    for (int i = 0; i < 4; ++i) glds16(xrows + st * 128, xoff + i * 8 * D_ * 2, la + slot * 4096 + i * 1024);
  };
  issueA(0, 0); issueA(1, 1); issueA(2, 2);
  const int a0 = r16 * 128 + ((g ^ (r16 & 7)) << 4);
  const int b0 = (8 * g + q) * 128 + (swz(8 * g + q) << 5) + p * 8;
  const int b1 = (8 * g + 4 + q) * 128 + (swz(8 * g + 4 + q) << 5) + p * 8;
  bf16x8 ones;
#pragma unroll
  for (int i = 0; i < 8; ++i) ones[i] = (bf16)1.0f;
  bf16x8 A[2][2], B[2][8], Bw[2];
  auto rdA = [&](const char* abase, const char* bbase, int u) {
    A[u][0] = ld_frag(abase + (a0 ^ (u << 6)));
    A[u][1] = ld_frag(abase + (a0 ^ (u << 6)) + 2048);
    if (STATS) Bw[u] = ld_tr2(bbase + 57344 + (b0 ^ (w << 5)), bbase + 57344 + (b1 ^ (w << 5)));
  };
  auto rdB = [&](const char* bbase, int u, int j) {
    const int o = (j >> 2) * 57344;
    B[u][j] = ld_tr2(bbase + o + (b0 ^ ((j & 3) << 5)), bbase + o + (b1 ^ ((j & 3) << 5)));
  };
  auto kstep = [&](int u, const char* nabase, const char* nbbase, bool more) {
#pragma unroll
    for (int j = 0; j < 8; ++j) {
      acc[0][j] = MFMA16(B[u][j], A[u][0], acc[0][j]);
      acc[1][j] = MFMA16(B[u][j], A[u][1], acc[1][j]);
      if (more) { if (j == 0) rdA(nabase, nbbase, u ^ 1); rdB(nbbase, u ^ 1, j); }
      if (STATS) {
        if (j == 1) sum[0] = MFMA16(ones, A[u][0], sum[0]);
        if (j == 2) sum[1] = MFMA16(ones, A[u][1], sum[1]);
        if (j == 4) sq[0] = MFMA16(A[u][0], A[u][0], sq[0]);
        if (j == 5) sq[1] = MFMA16(A[u][1], A[u][1], sq[1]);
        if (j == 7) cs = MFMA16(Bw[u], ones, cs);
      }
      SB();
    }
  };
  STAMP(1);
  WAIT_V(8);
  BAR(); STAMP(2);
  rdA(sa, smem, 0);
#pragma unroll
  for (int j = 0; j < 8; ++j) rdB(smem, 0, j);
  SB();
  int slot = 0;
#pragma unroll 1
  for (int s = 0; s < 6; ++s) {
    kstep(0, sa + slot * 4096, smem + s * 8192 + 4096, true);
    if (s < 5) WAIT_V(4); else WAIT_V(0);
    asm volatile("s_waitcnt lgkmcnt(14)" ::: "memory");
    __builtin_amdgcn_s_barrier();
    asm volatile("" ::: "memory");
    if (s < 4) issueA(s + 3, slot);
    slot = slot == 2 ? 0 : slot + 1;
    kstep(1, sa + slot * 4096, smem + s * 8192 + 8192, true);
  }
  kstep(0, sa + slot * 4096, smem + 6 * 8192 + 4096, true);
  kstep(1, sa, smem, false);
  STAMP(9);
}

__global__ __launch_bounds__(384, 2) void k1_qkv_h(const bf16* __restrict__ x, const bf16* __restrict__ wqkv,
                                                   const bf16* __restrict__ w1, const bf16* __restrict__ w2,
                                                   const bf16* __restrict__ w3, const bf16* __restrict__ w4,
                                                   const bf16* __restrict__ wo, const bf16* __restrict__ gw,
                                                   char* __restrict__ ws) {
  __shared__ __attribute__((aligned(16))) char smem[163840];
  const int tid = threadIdx.x, lane = tid & 63;
  const int w = __builtin_amdgcn_readfirstlane(tid >> 6);
  const int xcd = blockIdx.x & 7, idx = blockIdx.x >> 3;
  STAMP_DECL STAMP(0);
  if (idx >= 28) {
    const int hb = xcd * 4 + (idx - 28);
    float* red = (float*)smem;
    if (tid < 192) ((u32x4*)(ws + OFF_FL))[hb * 192 + tid] = u32x4{0u, 0u, 0u, 0u};
    if (w < 4) {
      if (hb < 28) {
        float a[8] = {};
#pragma unroll
        for (int i = 0; i < 14; ++i) {
          bf16x8 v = *(const bf16x8*)(w3 + (size_t)(w * 112 + i * 8 + (lane >> 3)) * F_ + hb * 64 + (lane & 7) * 8);
#pragma unroll
          for (int e = 0; e < 8; ++e) a[e] += (float)v[e];
        }
#pragma unroll
        for (int e = 0; e < 8; ++e) {
          a[e] += __shfl_xor(a[e], 8);
          a[e] = xsum4(a[e]);
        }
        if (lane < 8) {
#pragma unroll
          for (int e = 0; e < 8; ++e) red[w * 64 + lane * 8 + e] = a[e];
        }
      }
    } else {
      bf16x8 gv = {};
      if (lane < 56) gv = *(const bf16x8*)(gw + lane * 8);
      float* G = (float*)(ws + OFF_G);
#pragma unroll 1
      for (int rb = 0; rb < 2; ++rb) {
        bf16x8 xv[16];
        const int row0 = hb * 64 + (w - 4) * 32 + rb * 16;
#pragma unroll
        for (int r = 0; r < 16; ++r) {
          xv[r] = bf16x8{};
          if (lane < 56) xv[r] = *(const bf16x8*)(x + (row0 + r) * D_ + lane * 8);
        }
        float mine = 0.f;
#pragma unroll
        for (int r = 0; r < 16; ++r) {
          float s = 0.f;
#pragma unroll
          for (int i = 0; i < 4; ++i) {
            bf16x2 a = {xv[r][2 * i], xv[r][2 * i + 1]}, b = {gv[2 * i], gv[2 * i + 1]};
            s = __builtin_amdgcn_fdot2_f32_bf16(a, b, s, false);
          }
#pragma unroll
          for (int o = 8; o; o >>= 1) s += __shfl_xor(s, o);
          s = xsum4(s);
          mine = lane == r ? s : mine;
        }
        if (lane < 16) {
          f32x2 gg = {1.f / (1.f + __expf(-mine)), 1.f / (1.f + __expf(mine))};
          *(f32x2*)(G + (row0 + lane) * 2) = gg;
        }
      }
    }
    BAR();
    if (hb < 28 && tid < 64) ((float*)(ws + OFF_CS))[hb * 64 + tid] = (red[tid] + red[64 + tid]) + (red[128 + tid] + red[192 + tid]);
    STAMP_FLUSH(ws, 0);
    return;
  }
  const int panel = 4 * (xcd >> 1) + (idx & 3), ct = 7 * (xcd & 1) + (idx >> 2);
  const bool mixed = ct < 7;
  const int u0 = mixed ? ct : 2 * ct - 7;
  if (w >= 4) {
    const int r = lane >> 3;
    const bool second = w == 5;
    const unsigned l0 = lds_addr(smem) + (second ? 57344 : 0);
    const char* base = (second && mixed) ? (const char*)(w1 + ct * 64) : (const char*)(wqkv + (u0 + (second ? 1 : 0)) * 64);
    const int ld = (second && mixed) ? 896 : 2688;
    const unsigned cE = src_chunk(r, lane) * 16, cO = src_chunk(r + 8, lane) * 16;
    auto issue = [&](int st) {
#pragma unroll
      for (int i = 0; i < 8; ++i) glds16(base + (size_t)st * 64 * ld, (8 * i + r) * ld + ((i & 1) ? cO : cE), l0 + st * 8192 + i * 1024);
    };
    unsigned sink = 0;
    touch8k(sink, (const char*)(x + (size_t)panel * 128 * D_), (idx >> 2) * 2 + (w - 4), lane);
#pragma unroll 1
    for (int st = 0; st < 7; ++st) {
      if (st == 2) { WAIT_V(8); BAR(); }
      if (st == 5) { WAIT_V(24); BAR(); }
      issue(st);
    }
    WAIT_V(32); BAR();
    WAIT_V(24); BAR();
    WAIT_V(16); BAR();
    __builtin_amdgcn_s_sleep(4);
#pragma unroll
    for (int m = 0; m < 5; ++m) {
      const int c = idx * 2 + (w - 4) + 56 * m;
      touch8k(sink, c < 49 ? (const char*)w2 : (const char*)w3, c < 49 ? c : c < 245 ? c - 49 : 0, lane);
    }
    WAIT_V(13); BAR();
    WAIT_V(5); BAR();
    if (mixed) BAR();
    WAIT_V(0);
    touch_keep(sink);
    return;
  }
  const int g = lane >> 4, r16 = lane & 15;
  f32x4 acc[2][8] = {};
  f32x4 cs = {};
  f32x4 sum[2] = {}, sq[2] = {};
  float* ex = (float*)smem;
  const char* xrows = (const char*)(x + (size_t)panel * 128 * D_);
  if (mixed) {
    k1_compute<true>(smem, xrows, w, lane, acc, cs, sum, sq STAMP_PASS);
    if (r16 == 0) *(f32x4*)(ex + w * 16 + 4 * g) = cs;
  } else {
    k1_compute<false>(smem, xrows, w, lane, acc, cs, sum, sq STAMP_PASS);
  }
  {
    const int gl = (g >> 1) | ((g & 1) << 1);
#pragma unroll
    for (int jj = 0; jj < 4; ++jj) {
      if (jj < 2 || !mixed) {
#pragma unroll
        for (int rf = 0; rf < 2; ++rf) {
          const int tt = panel * 8 + 2 * w + rf;
          unsigned a0 = pack2(acc[rf][2 * jj][0], acc[rf][2 * jj][1]), a1 = pack2(acc[rf][2 * jj][2], acc[rf][2 * jj][3]);
          unsigned b0 = pack2(acc[rf][2 * jj + 1][0], acc[rf][2 * jj + 1][1]), b1 = pack2(acc[rf][2 * jj + 1][2], acc[rf][2 * jj + 1][3]);
          auto s0 = __builtin_amdgcn_permlane16_swap(a0, b0, false, false);
          auto s1 = __builtin_amdgcn_permlane16_swap(a1, b1, false, false);
          u32x4 v = {s0[0], s1[0], s0[1], s1[1]};
          *(u32x4*)(ws + OFF_QKV + ((size_t)((tt * 168 + u0 * 8 + 4 * jj + gl) * 16 + r16) << 4)) = v;
        }
      }
    }
  }
  if (!mixed) {
    unsigned sink = 0;
    const int c = ((xcd >> 1) * 28 + idx) * 4 + w;
    touch8k(sink, c < 49 ? (const char*)wo : (const char*)w4, c < 49 ? c : c < 245 ? c - 49 : 0, lane);
    WAIT_V(0); touch_keep(sink);
  } else {
    BAR();
    const int nb = ct * 64;
    f32x4 c4[4];
#pragma unroll
    for (int j = 0; j < 4; ++j) c4[j] = *(const f32x4*)(ex + j * 16 + 4 * g);
    unsigned hv[2][4][2];
#pragma unroll
    for (int rf = 0; rf < 2; ++rf) {
      float mean = sum[rf][0] * (1.f / D_);
      float dg = (r16 & 2) ? ((r16 & 1) ? sq[rf][3] : sq[rf][2]) : ((r16 & 1) ? sq[rf][1] : sq[rf][0]);
      dg = xsum4(g == (r16 >> 2) ? dg : 0.f);
      float var = fmaxf(dg * (1.f / D_) - mean * mean, 0.f);
      float a = __builtin_amdgcn_rsqf(var + 1e-5f);
      float k1 = GC1 * a, k3 = GC3 * a * a * a, inv = __builtin_amdgcn_rcpf(a);
      const f32x2 nm = {-mean, -mean};
#pragma unroll
      for (int j = 0; j < 4; ++j) {
        f32x2 z0 = __builtin_elementwise_fma(nm, f32x2{c4[j][0], c4[j][1]}, f32x2{acc[rf][4 + j][0], acc[rf][4 + j][1]});
        f32x2 z1 = __builtin_elementwise_fma(nm, f32x2{c4[j][2], c4[j][3]}, f32x2{acc[rf][4 + j][2], acc[rf][4 + j][3]});
        hv[rf][j][0] = pack2v(gelu2(z0, k1, k3, inv));
        hv[rf][j][1] = pack2v(gelu2(z1, k1, k3, inv));
      }
    }
#pragma unroll
    for (int rf = 0; rf < 2; ++rf)
#pragma unroll
      for (int j = 0; j < 4; ++j) asm volatile("" : "+v"(hv[rf][j][0]), "+v"(hv[rf][j][1]));
#pragma unroll
    for (int jj = 0; jj < 2; ++jj) {
      const int ks = (nb >> 5) + jj;
      int sh[2];
#pragma unroll
      for (int h = 0; h < 2; ++h) {
        const int grp = (nb + 32 * jj + 16 * h) / 112;
        sh[h] = (grp & 2 ? 2 : 1) * (grp & 1 ? -1 : 1);
      }
#pragma unroll
      for (int rf = 0; rf < 2; ++rf) {
        const int tok = panel * 128 + 32 * w + 16 * rf + r16;
        int t2[2];
        u32x2 v[2];
#pragma unroll
        for (int h = 0; h < 2; ++h) {
          t2[h] = tok + sh[h];
          const bool ok = (unsigned)t2[h] < (unsigned)S_;
          t2[h] &= S_ - 1;
          v[h][0] = ok ? hv[rf][2 * jj + h][0] : 0u;
          v[h][1] = ok ? hv[rf][2 * jj + h][1] : 0u;
        }
        char* d0 = ws + OFF_HS + ((size_t)(((t2[0] >> 4) * 14 + ks) * 64 + g * 16 + (t2[0] & 15)) << 4);
        if (ks != 3 && ks != 10) {
          u32x4 vv = {v[0][0], v[0][1], v[1][0], v[1][1]};
          *(u32x4*)d0 = vv;
        } else {
          char* d1 = ws + OFF_HS + ((size_t)(((t2[1] >> 4) * 14 + ks) * 64 + g * 16 + (t2[1] & 15)) << 4) + 8;
          *(u32x2*)d0 = v[0];
          *(u32x2*)d1 = v[1];
        }
      }
    }
  }
  STAMP(10);
  STAMP_FLUSH(ws, 0);
}

typedef __attribute__((address_space(1))) unsigned gu32;
typedef __amdgpu_buffer_rsrc_t rsrc_t;
DI u32x4 ld16_sc1(rsrc_t r, unsigned off) { return __builtin_amdgcn_raw_buffer_load_b128(r, off, 0, 16); }
DI u32x4 ld16(rsrc_t r, unsigned off) { return __builtin_amdgcn_raw_buffer_load_b128(r, off, 0, 0); }
DI void st16_sc1(rsrc_t r, unsigned off, u32x4 v) { __builtin_amdgcn_raw_buffer_store_b128(v, r, off, 0, 16); }
DI void st8_sc1(rsrc_t r, unsigned off, u32x2 v) { __builtin_amdgcn_raw_buffer_store_b64(v, r, off, 0, 16); }
DI bf16x8 as_frag(u32x4 v) { return __builtin_bit_cast(bf16x8, v); }
DI void post_flags(char* ws, int kind, int consumer, int word, int lane) {
  if (lane < 14)
    __hip_atomic_store((gu32*)(ws + OFF_FL) + (kind * 256 + consumer) * 32 + word, 1u, __ATOMIC_RELAXED, __HIP_MEMORY_SCOPE_AGENT);
}
DI void wait_flags(char* ws, int kind, int nwords, int lane) {
  gu32* f = (gu32*)(ws + OFF_FL) + (kind * 256 + blockIdx.x) * 32 + (lane < nwords ? lane : 0);
#pragma unroll 1
  for (int spin = 0; spin < (1 << 18); ++spin) {
    unsigned v = __hip_atomic_load(f, __ATOMIC_RELAXED, __HIP_MEMORY_SCOPE_AGENT);
    if (__all(v == 1u)) break;
    __builtin_amdgcn_s_sleep(2);
  }
  asm volatile("" ::: "memory");
}
constexpr int L_H1 = 57344, L_X = 102400, L_EV = 110592, L_C = 114688;
DI void ev_signal(char* smem, int ev, int wq, int lane) {
  asm volatile("s_waitcnt lgkmcnt(0)" ::: "memory");
  if (lane == 0) *(volatile LDS_AS unsigned*)(LDS_AS char*)(smem + L_EV + ev * 16 + wq * 4) = 1u;
  asm volatile("" ::: "memory");
}
DI void ev_wait(char* smem, int ev) {
  asm volatile("" ::: "memory");
#pragma unroll 1
  for (int spin = 0; spin < (1 << 22); ++spin) {
    u32x4 v = *(volatile LDS_AS u32x4*)(LDS_AS char*)(smem + L_EV + ev * 16);
    if (__builtin_amdgcn_readfirstlane(v[0] & v[1] & v[2] & v[3])) break;
    __builtin_amdgcn_s_sleep(1);
  }
  asm volatile("" ::: "memory");
}
DI void ev_signal_all(char* smem, int ev, int lane) {
  asm volatile("" ::: "memory");
  if (lane < 4) *(volatile LDS_AS unsigned*)(LDS_AS char*)(smem + L_EV + ev * 16 + lane * 4) = 1u;
  asm volatile("" ::: "memory");
}
DI int slotA(int c, int m) {
  if (m < 4) { const int n = 4 * c + m; return n < 8 ? n * 4096 : L_H1 + (n - 8) * 4096; }
  const int n = 3 * c + m - 4;
  return n < 6 ? 32768 + n * 4096 : n < 11 ? 90112 + (n - 6) * 4096 : L_C;
}

__global__ __launch_bounds__(512, 2) void k2_mega(const bf16* __restrict__ x, const bf16* __restrict__ w2,
                                                  const bf16* __restrict__ w3, const bf16* __restrict__ w4,
                                                  const bf16* __restrict__ wo, char* __restrict__ ws,
                                                  bf16* __restrict__ out) {
  __shared__ __attribute__((aligned(16))) char smem[163840];
  const int tid = threadIdx.x, lane = tid & 63;
  const int w8 = __builtin_amdgcn_readfirstlane(tid >> 6);
  const int grp = w8 >> 2, w = w8 & 3;
  const int xcd = blockIdx.x & 7, idx = blockIdx.x >> 3;
  STAMP_DECL STAMP(0);
  const unsigned l0 = lds_addr(smem);
  const bool has_gemm = idx < 28;
  const int rt64 = 4 * xcd + (idx & 3), c7 = idx >> 2;
  const int panel = 2 * xcd + (idx & 1), c14 = idx >> 1;
  const int head = c14;
  const int g = lane >> 4, q = (lane >> 2) & 3, p = lane & 3, r16 = lane & 15, r8 = lane >> 3;
  const rsrc_t rs = __builtin_amdgcn_make_buffer_rsrc(ws, 0, (int)WS_USED, 0x00020000);
  const int btr = (4 * g + q) * 128 + (swz(4 * g + q) << 5) + p * 8;
  const unsigned cE = src_chunk(r8, lane) * 16, cO = src_chunk(r8 + 8, lane) * 16;
  auto loadW = [&](const char* base, int ld, int g0, int n, unsigned dst) {
    const unsigned oE = r8 * ld + cE, oO = (r8 + 8) * ld + cO;
#pragma unroll 1
    for (int m = g0; m < g0 + n; ++m) {
      glds16(base + (size_t)m * 16 * ld, oE, dst + m * 2048);
      glds16(base + (size_t)m * 16 * ld, oO, dst + m * 2048 + 1024);
    }
  };
  const char* w3b = (const char*)(w3 + c14 * 128);
  const char* wob = (const char*)(wo + c7 * 64);
  const char* w4b = (const char*)(w4 + c7 * 64);
  if (tid < 64) ((unsigned*)(smem + L_EV))[tid] = 0u;
  const int cons3 = ((2 * lane + ((rt64 >> 1) & 1)) << 3) | xcd;
  const int cons4 = ((4 * (lane >> 1) + 2 * (panel & 1) + (lane & 1)) << 3) | xcd;

  if (grp == 0) {
    if (!has_gemm) { BAR(); return; }
    __builtin_amdgcn_s_setprio(2);
    loadW((const char*)(w2 + c7 * 64), D_ * 2, 7 * w, 7, l0);
    {
      const char* bx = (const char*)(x + (size_t)(rt64 * 64 + 16 * w) * D_ + c7 * 64);
#pragma unroll
      for (int i = 0; i < 2; ++i) glds16(bx, ((8 * i + r8) * D_ + (((lane & 7) ^ r8) << 3)) * 2, l0 + L_X + w * 2048 + i * 1024);
    }
    u32x4 ha[14];
#pragma unroll
    for (int ks = 0; ks < 14; ++ks) ha[ks] = ld16(rs, OFF_HS + (((rt64 * 4 + w) * 14 + ks) * 64 + lane) * 16);
    BAR();
    WAIT_V(14);
    ev_signal(smem, 0, w, lane);
    ev_wait(smem, 0);
    STAMP(1);
    {
      f32x4 acc[4] = {};
      bf16x8 bq[3][4];
      auto rdB = [&](int ks, int j) {
        const char* bp = smem + ks * 4096 + (btr ^ (j << 5));
        bq[ks % 3][j] = ld_tr2(bp, bp + 2048);
      };
#pragma unroll
      for (int j = 0; j < 4; ++j) { rdB(0, j); rdB(1, j); }
      const int trow = 16 * w + r16;
      u32x2 xr[4];
      SB();
#pragma unroll
      for (int ks = 0; ks < 14; ++ks) {
#pragma unroll
        for (int j = 0; j < 4; ++j) {
          acc[j] = MFMA16(bq[ks % 3][j], as_frag(ha[ks]), acc[j]);
          if (ks + 2 < 14) rdB(ks + 2, j);
          if (ks == 12) xr[j] = *(const u32x2*)(smem + L_X + trow * 128 + (((2 * j + (g >> 1)) ^ (trow & 7)) << 4) + (g & 1) * 8);
          SB();
        }
      }
      STAMP(11);
      float sum = 0.f, sq = 0.f;
      u32x4 x1k[2];
#pragma unroll
      for (int j = 0; j < 4; ++j) {
        unsigned lo = pack2(acc[j][0] + bf_lo(xr[j][0]), acc[j][1] + bf_hi(xr[j][0]));
        unsigned hi = pack2(acc[j][2] + bf_lo(xr[j][1]), acc[j][3] + bf_hi(xr[j][1]));
        x1k[j >> 1][2 * (j & 1)] = lo; x1k[j >> 1][2 * (j & 1) + 1] = hi;
        float e0 = bf_lo(lo), e1 = bf_hi(lo), e2 = bf_lo(hi), e3 = bf_hi(hi);
        sum += (e0 + e1) + (e2 + e3);
        sq += (e0 * e0 + e1 * e1) + (e2 * e2 + e3 * e3);
      }
#pragma unroll
      for (int jj = 0; jj < 2; ++jj) st16_sc1(rs, OFF_X1 + (((rt64 * 4 + w) * 14 + 2 * c7 + jj) * 64 + lane) * 16, x1k[jj]);
      u32x2 t = {__float_as_uint(xsum4(sum)), __float_as_uint(xsum4(sq))};
      if (g == 0) st8_sc1(rs, OFF_ST + (rt64 * 4 + w) * 1024 + (c7 >> 1) * 256 + r16 * 16 + (c7 & 1) * 8, t);
    }
    WAIT_V(0);
    ev_signal(smem, 8, w, lane);
    ev_wait(smem, 8);
    if (w == 0) post_flags(ws, 0, cons3, (rt64 & 1) * 7 + c7, lane);
    STAMP(2);
    loadW(w3b, F_ * 2, 7 * w, 7, l0);
    loadW(w3b + 128, F_ * 2, 22 + w, 1, l0 + L_H1);
    WAIT_V(0);
    STAMP(3);
    ev_signal(smem, 2, w, lane);
    if (w == 0) {
      wait_flags(ws, 0, 14, lane);
      ev_signal_all(smem, 9, lane);
    } else {
      ev_wait(smem, 9);
    }
    STAMP(4);
    {
      u32x4 a[2][14];
      const unsigned aoff = OFF_X1 + ((panel * 8 + 2 * w) * 14 * 64 + lane) * 16;
      auto ldA = [&](int ks) {
        a[0][ks] = ld16_sc1(rs, aoff + ks * 1024);
        a[1][ks] = ld16_sc1(rs, aoff + 14336 + ks * 1024);
      };
#pragma unroll
      for (int ks = 0; ks < 6; ++ks) ldA(ks);
      ev_wait(smem, 2);
      ev_wait(smem, 6);
      STAMP(5);
      u32x4 stv[2];
      float g1[2];
      f32x4 c4[8];
      f32x4 acc[2][8] = {};
      bf16x8 bq[8];
      auto rdB = [&](int n) {
        const int ks = n >> 3, j = n & 7;
        const int base = j < 4 ? ks * 4096 : (ks < 13 ? L_H1 + ks * 4096 : L_C);
        const char* bp = smem + base + (btr ^ ((j & 3) << 5));
        return ld_tr2(bp, bp + 2048);
      };
#pragma unroll
      for (int n = 0; n < 6; ++n) bq[n] = rdB(n);
      SB();
#pragma unroll
      for (int ks = 0; ks < 14; ++ks) {
        if (ks == 12) ev_wait(smem, 7);
        if (ks == 9 && lane == 0) *(volatile LDS_AS unsigned*)(LDS_AS char*)(smem + L_EV + 15 * 16 + w * 4) = 1u;
#pragma unroll
        for (int j = 0; j < 8; ++j) {
          const int n = ks * 8 + j;
          acc[0][j] = MFMA16(bq[j], as_frag(a[0][ks]), acc[0][j]);
          acc[1][j] = MFMA16(bq[j], as_frag(a[1][ks]), acc[1][j]);
          if (n + 6 < 112) bq[(j + 6) & 7] = rdB(n + 6);
          if (j == 1 && ks + 6 < 14) ldA(ks + 6);
          if (ks == 8 && j < 2) {
            stv[j] = ld16_sc1(rs, OFF_ST + (panel * 8 + 2 * w + j) * 1024 + lane * 16);
            g1[j] = ((const float*)(ws + OFF_G))[(panel * 128 + 32 * w + 16 * j + r16) * 2 + 1];
          }
          if (ks == 9) c4[j] = *(const f32x4*)(ws + OFF_CS + (size_t)(c14 * 128 + 16 * j + 4 * g) * 4);
          SB();
        }
      }
      ev_signal(smem, 3, w, lane);
      STAMP(6);
#pragma unroll
      for (int rf = 0; rf < 2; ++rf) {
        float s1 = __uint_as_float(stv[rf][0]), s2 = __uint_as_float(stv[rf][1]);
        if (g < 3) { s1 += __uint_as_float(stv[rf][2]); s2 += __uint_as_float(stv[rf][3]); }
        s1 = xsum4(s1); s2 = xsum4(s2);
        float mean = s1 * (1.f / D_);
        float var = fmaxf(s2 * (1.f / D_) - mean * mean, 0.f);
        float a_ = __builtin_amdgcn_rsqf(var + 1e-5f);
        float k1 = GC1 * a_, k3 = GC3 * a_ * a_ * a_, inv = __builtin_amdgcn_rcpf(fmaxf(a_ * g1[rf], 1e-30f));
        const f32x2 nm = {-mean, -mean};
#pragma unroll
        for (int jj = 0; jj < 4; ++jj) {
          u32x4 v;
#pragma unroll
          for (int h = 0; h < 2; ++h) {
            const int j = 2 * jj + h;
            f32x2 z0 = __builtin_elementwise_fma(nm, f32x2{c4[j][0], c4[j][1]}, f32x2{acc[rf][j][0], acc[rf][j][1]});
            f32x2 z1 = __builtin_elementwise_fma(nm, f32x2{c4[j][2], c4[j][3]}, f32x2{acc[rf][j][2], acc[rf][j][3]});
            v[2 * h] = pack2v(gelu2(z0, k1, k3, inv));
            v[2 * h + 1] = pack2v(gelu2(z1, k1, k3, inv));
          }
          st16_sc1(rs, OFF_AC + (((panel * 8 + 2 * w + rf) * 72 + 16 + c14 * 4 + jj) * 64 + lane) * 16, v);
        }
      }
    }
    STAMP(7);
    WAIT_V(0);
    STAMP(12);
    ev_signal(smem, 10, w, lane);
    if (w == 0) {
      ev_wait(smem, 10);
      post_flags(ws, 2, cons4, c14, lane);
    }
    __builtin_amdgcn_s_setprio(0);
  } else {
    const int gb = (7 * head) >> 1;
    const int qw = panel * 128 + 32 * w;
    BAR();
    {
      const unsigned lv = (lane & 48) | ((lane & 15) ^ ((lane & 16) >> 1));
      const char* src = ws + OFF_QKV + (size_t)(56 + gb) * 256;
#pragma unroll
      for (int m = 0; m < 6; ++m) {
        const int lt = 6 * w + m;
        int kt = panel * 8 - 16 + lt;
        kt = kt < 0 ? 0 : kt;
        glds16(src + (size_t)kt * 43008, lane * 16, l0 + L_C + lt * 1024);
        glds16(src + (size_t)kt * 43008 + 56 * 256, lv * 16, l0 + L_C + 24576 + lt * 1024);
      }
    }
    u32x4 qv[2];
#pragma unroll
    for (int h = 0; h < 2; ++h)
      asm volatile("global_load_dwordx4 %0, %1, %2" : "=v"(qv[h]) : "v"((((panel * 8 + 2 * w + h) * 168 + gb) * 16 + lane) * 16), "s"(ws + OFF_QKV) : "memory");
    if (has_gemm) {
      const unsigned o0 = (88 * w + r8) * (F_ * 2) + ((w & 1) ? cO : cE), o1 = (88 * w + r8) * (F_ * 2) + ((w & 1) ? cE : cO);
      const unsigned ld = l0 + L_H1 + w * 11264;
#pragma unroll
      for (int m = 0; m < 11; ++m) glds16(w3b + 128, ((m & 1) ? o1 : o0) + m * 8 * F_ * 2, ld + m * 1024);
      WAIT_V(11);
    } else {
      WAIT_V(0);
    }
    asm volatile("" : "+v"(qv[0]), "+v"(qv[1])::"memory");
    ev_signal(smem, 4, w, lane);
    ev_wait(smem, 4);
    STAMP(1);
    const char* sK = smem + L_C;
    const char* sV = smem + L_C + 24576;
    const bool odd = head & 1;
    const bool z01 = odd && g == 0, z23 = !odd && g == 3;
    bf16x8 qf[2];
#pragma unroll
    for (int h = 0; h < 2; ++h) {
      qv[h][0] = z01 ? 0u : qv[h][0]; qv[h][1] = z01 ? 0u : qv[h][1];
      qv[h][2] = z23 ? 0u : qv[h][2]; qv[h][3] = z23 ? 0u : qv[h][3];
      qf[h] = as_frag(qv[h]);
    }
    const float NEG = -__builtin_inff();
    constexpr float SC = 0.1889822365046136f * 1.4426950408889634f;
    f32x4 s[2][17];
    float mx[2] = {NEG, NEG};
    auto max4 = [&](float m, const f32x4& v) {
      return __builtin_fmaxf(__builtin_fmaxf(__builtin_fmaxf(__builtin_fmaxf(m, v[0]), v[1]), v[2]), v[3]);
    };
    auto finish = [&](int h, int t) {
      if (t == 0 || t == 16) {
#pragma unroll
        for (int i = 0; i < 4; ++i) {
          const int b = 4 * g + i;
          s[h][t][i] = ((b > r16) == (t == 0)) ? s[h][t][i] : NEG;
        }
      }
      mx[h] = max4(mx[h], s[h][t]);
    };
    {
      bf16x8 kf[6];
#pragma unroll
      for (int t = 0; t < 6; ++t) kf[t] = ld_frag(sK + (2 * w + t) * 1024 + lane * 16);
      SB();
#pragma unroll
      for (int t = 0; t < 20; ++t) {
        if (t < 18) {
          f32x4 z = {};
          if (t < 17) s[0][t] = MFMA16(kf[t % 6], qf[0], z);
          if (t > 0) s[1][t - 1] = MFMA16(kf[t % 6], qf[1], z);
          if (t + 6 < 18) kf[t % 6] = ld_frag(sK + (2 * w + t + 6) * 1024 + lane * 16);
        }
        if (t >= 2) {
          if (t - 2 < 17) finish(0, t - 2);
          if (t - 2 > 0) finish(1, t - 3);
        }
        SB();
      }
    }
    if (qw < 256) {
#pragma unroll
      for (int h = 0; h < 2; ++h) {
        const int tstart = 16 - (qw >> 4) - h;
        mx[h] = NEG;
#pragma unroll
        for (int t = 0; t < 17; ++t) {
          if (t < tstart) s[h][t] = f32x4{NEG, NEG, NEG, NEG};
          mx[h] = max4(mx[h], s[h][t]);
        }
      }
    }
    const float mc[2] = {-xmax4(mx[0]) * SC, -xmax4(mx[1]) * SC};
    STAMP(2);
    WAIT_V(0);
    ev_signal(smem, 6, w, lane);
    float gt[2];
#pragma unroll
    for (int h = 0; h < 2; ++h) gt[h] = ((const float*)(ws + OFF_G))[(qw + 16 * h + r16) * 2];
    STAMP(3);
    f32x4 o[2][2] = {}, lsum[2] = {};
    bf16x8 ones;
#pragma unroll
    for (int i = 0; i < 8; ++i) ones[i] = (bf16)1.0f;
    auto pexp = [&](const f32x4& sv, float m, unsigned& lo, unsigned& hi) {
      f32x2 a = __builtin_elementwise_fma(f32x2{sv[0], sv[1]}, f32x2{SC, SC}, f32x2{m, m});
      f32x2 b = __builtin_elementwise_fma(f32x2{sv[2], sv[3]}, f32x2{SC, SC}, f32x2{m, m});
      lo = pack2(__builtin_amdgcn_exp2f(a[0]), __builtin_amdgcn_exp2f(a[1]));
      hi = pack2(__builtin_amdgcn_exp2f(b[0]), __builtin_amdgcn_exp2f(b[1]));
    };
    const int voff0 = (p >> 1) * 256 + (((4 * g + q) ^ ((p >> 1) << 3)) << 4) + (p & 1) * 8;
    bf16x8 vf[2][2], pf[2][2];
#pragma unroll
    for (int kk = 0; kk < 10; ++kk) {
      if (kk < 9) {
        const char* vb = sV + (2 * w + 2 * kk) * 1024 + voff0;
        vf[kk & 1][0] = ld_tr2(vb, vb + 1024);
        vf[kk & 1][1] = ld_tr2(vb + 512, vb + 1536);
#pragma unroll
        for (int h = 0; h < 2; ++h) {
          unsigned p0 = 0, p1 = 0, p2 = 0, p3 = 0;
          const int t0 = 2 * kk - h, t1 = t0 + 1;
          if (t0 >= 0) pexp(s[h][t0 < 0 ? 0 : t0], mc[h], p0, p1);
          if (t1 <= 16) pexp(s[h][t1 > 16 ? 16 : t1], mc[h], p2, p3);
          pf[kk & 1][h] = as_frag(u32x4{p0, p1, p2, p3});
        }
      }
      if (kk > 0) {
        const int c = (kk - 1) & 1;
#pragma unroll
        for (int h = 0; h < 2; ++h) {
          o[h][0] = MFMA16(vf[c][0], pf[c][h], o[h][0]);
          o[h][1] = MFMA16(vf[c][1], pf[c][h], o[h][1]);
          lsum[h] = MFMA16(ones, pf[c][h], lsum[h]);
        }
      }
      SGB(0x100, 4);
#pragma unroll
      for (int m = 0; m < 6; ++m) { SGB(0x8, 1); SGB(0x402, 5); }
      SB();
    }
    ev_signal(smem, 5, w, lane);
    STAMP(4);
#pragma unroll
    for (int h = 0; h < 2; ++h) {
      float sc = gt[h] * __builtin_amdgcn_rcpf(lsum[h][0]);
      u32x4 v;
      v[0] = z01 ? 0u : pack2(o[h][0][0] * sc, o[h][0][1] * sc);
      v[1] = z01 ? 0u : pack2(o[h][0][2] * sc, o[h][0][3] * sc);
      v[2] = z23 ? 0u : pack2(o[h][1][0] * sc, o[h][1][1] * sc);
      v[3] = z23 ? 0u : pack2(o[h][1][2] * sc, o[h][1][3] * sc);
      st16_sc1(rs, OFF_AC + (((panel * 8 + 2 * w + h) * 72 + head) * 64 + lane) * 16, v);
    }
    if (!has_gemm) {
      WAIT_V(0);
      ev_signal(smem, 11, w, lane);
      if (w == 0) {
        ev_wait(smem, 11);
        post_flags(ws, 1, cons4, head, lane);
      }
      return;
    }
    ev_wait(smem, 5);
    glds16(w3b + 128 + (size_t)(416 + 8 * w) * F_ * 2, r8 * (F_ * 2) + ((w & 1) ? cO : cE), l0 + L_C + w * 1024);
  }
  {
    unsigned aoff[4];
#pragma unroll
    for (int rt = 0; rt < 4; ++rt) aoff[rt] = OFF_AC + (((rt64 * 4 + (rt ^ (w8 >> 1))) * 72 + w) * 64 + lane) * 16;
    const int bx = btr ^ ((w8 & 1) << 6);
    f32x4 acc[4][4] = {};
    u32x4 a[3][4];
    bf16x8 bp[7][4];
    auto ldA = [&](int buf, int k4) {
#pragma unroll
      for (int rt = 0; rt < 4; ++rt) a[buf][rt] = ld16_sc1(rs, aoff[rt] + k4 * 4096);
    };
    auto rdB = [&](bf16x8 (&bf)[4], const char* base) {
#pragma unroll
      for (int j = 0; j < 4; ++j) bf[j] = ld_tr2(base + (bx ^ (j << 5)), base + (bx ^ (j << 5)) + 2048);
    };
    auto mma = [&](const u32x4 (&af)[4], const bf16x8 (&bf)[4]) {
#pragma unroll
      for (int rt = 0; rt < 4; ++rt)
#pragma unroll
        for (int j = 0; j < 4; ++j) acc[rt][j] = MFMA16(bf[j], as_frag(af[rt]), acc[rt][j]);
    };
    unsigned v4[4];
#pragma unroll
    for (int i = 0; i < 4; ++i) v4[i] = (32 * w + 8 * i + r8) * (D_ * 2) + ((i & 1) ? cO : cE);
    auto loadA4 = [&](int m) {
      const char* src = w4b + (size_t)(28 * 32 + m * 128) * D_ * 2;
      const unsigned ld = l0 + slotA(w, m);
      glds16c<0>(src, v4[0], ld); glds16c<1024>(src, v4[1], ld); glds16c<2048>(src, v4[2], ld); glds16c<3072>(src, v4[3], ld);
    };
    if (grp == 1) {
      unsigned vo[4];
#pragma unroll
      for (int i = 0; i < 4; ++i) {
        int d = 8 * i + r8 - 4 * (w & 1);
        d = d < 0 ? 0 : (d > 27 ? 27 : d);
        vo[i] = (28 * w + d) * (D_ * 2) + ((i & 1) ? cO : cE);
      }
      const unsigned lst = l0 + L_C + 4096 + w * 11264;
      const char* sst = smem + L_C + 4096 + w * 11264;
      auto issue = [&](int n) {
        const bool o_ = n >= 6 && n < 10;
        const char* src = o_ ? wob + (size_t)(n - 6) * 112 * D_ * 2 : w4b + (size_t)(n < 6 ? n : 6) * 128 * D_ * 2;
        if (n & 1) {
          glds16c<4096>(src, o_ ? vo[0] : v4[0], lst); glds16c<5120>(src, o_ ? vo[1] : v4[1], lst);
          glds16c<6144>(src, o_ ? vo[2] : v4[2], lst); glds16c<7168>(src, o_ ? vo[3] : v4[3], lst);
        } else {
          glds16c<0>(src, o_ ? vo[0] : v4[0], lst); glds16c<1024>(src, o_ ? vo[1] : v4[1], lst);
          glds16c<2048>(src, o_ ? vo[2] : v4[2], lst); glds16c<3072>(src, o_ ? vo[3] : v4[3], lst);
        }
      };
      issue(0); issue(1);
      WAIT_V(8);
      ev_signal(smem, 7, w, lane);
      STAMP(5);
      if (w == 0) {
        ev_wait(smem, 7);
        post_flags(ws, 1, cons4, head, lane);
        WAIT_V(0);
      }
#pragma unroll
      for (int n = 0; n < 6; ++n) {
        WAIT_V(4);
        rdB(bp[n], sst + (n & 1) * 4096);
        asm volatile("s_waitcnt lgkmcnt(0)" ::: "memory");
        issue(n + 2);
      }
      STAMP(6);
      ev_wait(smem, 15);
#pragma unroll
      for (int m = 0; m < 4; ++m) loadA4(m);
      if (w == 0) {
        wait_flags(ws, 1, 16, lane);
        ev_signal_all(smem, 12, lane);
      } else {
        ev_wait(smem, 12);
      }
      STAMP(7);
      ldA(0, 0); ldA(1, 1);
      WAIT_V(8);
      ev_signal(smem, 14, w, lane);
      rdB(bp[6], sst);
      asm volatile("s_waitcnt lgkmcnt(0)" ::: "memory");
      WAIT_V(0);
      issue(8);
      mma(a[0], bp[6]);
      SB();
      rdB(bp[6], sst + 4096);
      asm volatile("s_waitcnt lgkmcnt(0)" ::: "memory");
      issue(9);
      mma(a[1], bp[6]);
      SB();
      ldA(0, 2); ldA(1, 3);
      WAIT_V(8);
      rdB(bp[6], sst);
      asm volatile("s_waitcnt lgkmcnt(0)" ::: "memory");
      WAIT_V(0);
      issue(10);
      mma(a[0], bp[6]);
      SB();
      rdB(bp[6], sst + 4096);
      mma(a[1], bp[6]);
      SB();
      STAMP(8);
      WAIT_V(0);
      rdB(bp[6], sst);
      STAMP(12);
    } else {
      ev_wait(smem, 3);
#pragma unroll
      for (int m = 4; m < 7; ++m) loadA4(m);
      ev_wait(smem, 14);
#pragma unroll
      for (int m = 0; m < 4; ++m) rdB(bp[m], smem + slotA(w, m));
      WAIT_V(0);
#pragma unroll
      for (int m = 4; m < 7; ++m) rdB(bp[m], smem + slotA(w, m));
      STAMP(8);
      if (w == 0) {
        wait_flags(ws, 2, 14, lane);
        ev_signal_all(smem, 13, lane);
      }
    }
    ev_wait(smem, 13);
    STAMP(9);
    const int k0 = grp ? 4 : 11;
#pragma unroll
    for (int m = 0; m < 3; ++m) ldA(m, k0 + m);
    const int tt = rt64 * 4 + (w8 >> 1);
    u32x4 x1v;
    float g1o;
    SB();
#pragma unroll
    for (int m = 0; m < 7; ++m) {
      mma(a[m % 3], bp[m]);
      SB();
      if (m + 3 < 7) ldA(m % 3, k0 + m + 3);
      if (m == 4) {
        x1v = ld16_sc1(rs, OFF_X1 + ((tt * 14 + 2 * c7 + (w8 & 1)) * 64 + lane) * 16);
        g1o = ((const float*)(ws + OFF_G))[(tt * 16 + r16) * 2 + 1];
      }
      SB();
    }
    STAMP(10);
    BAR();
    f32x4* ex = (f32x4*)smem;
#pragma unroll
    for (int pos = 1; pos < 8; ++pos)
#pragma unroll
      for (int h = 0; h < 2; ++h) ex[(((w8 ^ pos) * 7 + pos - 1) * 2 + h) * 64 + lane] = acc[pos >> 1][2 * (pos & 1) + h];
    BAR();
    STAMP(13);
    unsigned pk[2][2];
#pragma unroll
    for (int h = 0; h < 2; ++h) {
      f32x4 r = acc[0][h];
#pragma unroll
      for (int sidx = 0; sidx < 7; ++sidx) r += ex[((w8 * 7 + sidx) * 2 + h) * 64 + lane];
      pk[h][0] = pack2(__builtin_fmaf(g1o, bf_lo(x1v[2 * h]), r[0]), __builtin_fmaf(g1o, bf_hi(x1v[2 * h]), r[1]));
      pk[h][1] = pack2(__builtin_fmaf(g1o, bf_lo(x1v[2 * h + 1]), r[2]), __builtin_fmaf(g1o, bf_hi(x1v[2 * h + 1]), r[3]));
    }
    auto s0 = __builtin_amdgcn_permlane16_swap(pk[0][0], pk[1][0], false, false);
    auto s1 = __builtin_amdgcn_permlane16_swap(pk[0][1], pk[1][1], false, false);
    u32x4 v = {s0[0], s1[0], s0[1], s1[1]};
    const int gl = (g >> 1) | ((g & 1) << 1);
    *(u32x4*)(out + (size_t)(tt * 16 + r16) * D_ + c7 * 64 + 32 * (w8 & 1) + 8 * gl) = v;
  }
  STAMP_FLUSH(ws, 1);
}

extern "C" void kernel_launch(void* const* d_in, const int* in_sizes, int n_in, void* d_out, int out_size, void* d_ws,
                              size_t ws_size, hipStream_t stream) {
  const bf16* x = (const bf16*)d_in[0];
  const bf16* wqkv = (const bf16*)d_in[1];
  const bf16* wo = (const bf16*)d_in[2];
  const bf16* w1 = (const bf16*)d_in[4];
  const bf16* w2 = (const bf16*)d_in[5];
  const bf16* w3 = (const bf16*)d_in[6];
  const bf16* w4 = (const bf16*)d_in[7];
  const bf16* gw = (const bf16*)d_in[8];
  char* ws = (char*)d_ws;
  k1_qkv_h<<<256, 384, 0, stream>>>(x, wqkv, w1, w2, w3, w4, wo, gw, ws);
  k2_mega<<<256, 512, 0, stream>>>(x, w2, w3, w4, wo, ws, (bf16*)d_out);
}
```

```cpp
#include <hip/hip_runtime.h>
#include <hip/hip_bf16.h>

typedef __bf16 bf16;
typedef __bf16 bf16x2 __attribute__((ext_vector_type(2)));
typedef __bf16 bf16x4 __attribute__((ext_vector_type(4)));
typedef __bf16 bf16x8 __attribute__((ext_vector_type(8)));
typedef float f32x2 __attribute__((ext_vector_type(2)));
typedef float f32x4 __attribute__((ext_vector_type(4)));
typedef unsigned u32x2 __attribute__((ext_vector_type(2)));
typedef unsigned u32x4 __attribute__((ext_vector_type(4)));
#define LDS_AS __attribute__((address_space(3)))
#define DI __device__ __forceinline__

constexpr int S_ = 2048, D_ = 448, F_ = 1792;
constexpr size_t OFF_QKV = 0;
constexpr size_t OFF_HS = OFF_QKV + 5505024;
constexpr size_t OFF_X1 = OFF_HS + 1835008;
constexpr size_t OFF_AC = OFF_X1 + 1835008;
constexpr size_t OFF_ST = OFF_AC + 9437184;
constexpr size_t OFF_G  = OFF_ST + 262144;
constexpr size_t OFF_CS = OFF_G + 16384;
constexpr size_t OFF_FL = OFF_CS + 8192;
constexpr size_t WS_USED = OFF_FL + 3 * 256 * 128;

#define STAMP_DECL
#define STAMP(i)
#define STAMP_FLUSH(ws, kid)
#define STAMP_ARG
#define STAMP_PASS

#define WAIT_V(n) asm volatile("s_waitcnt vmcnt(" #n ")" ::: "memory")
#define BAR() do { asm volatile("s_waitcnt lgkmcnt(0)" ::: "memory"); __builtin_amdgcn_s_barrier(); asm volatile("" ::: "memory"); } while (0)
#define MFMA16(a, b, c) __builtin_amdgcn_mfma_f32_16x16x32_bf16((a), (b), (c), 0, 0, 0)

template <int IMM = 0>
DI void glds16(const void* sbase, unsigned voff, unsigned lds) {
  asm volatile("s_mov_b32 m0, %2\n\ts_nop 0\n\tglobal_load_lds_dwordx4 %0, %1 offset:%3" ::"v"(voff), "s"(sbase), "s"(lds), "i"(IMM) : "memory");
}
template <int LOFF>
DI void glds16c(const void* sbase, unsigned voff, unsigned ldsbase) {
  asm volatile("s_add_u32 m0, %2, %3\n\ts_nop 0\n\tglobal_load_lds_dwordx4 %0, %1" ::"v"(voff), "s"(sbase), "s"(ldsbase), "i"(LOFF) : "memory", "scc");
}
DI void touch8k(unsigned& sink, const void* sbase, unsigned chunk, int lane) {
  asm volatile("global_load_dword %0, %1, %2" : "+v"(sink) : "v"(chunk * 8192u + lane * 128u), "s"(sbase) : "memory");
}
DI void touch_keep(unsigned& sink) { asm volatile("" : "+v"(sink)::"memory"); }
DI unsigned lds_addr(const char* p) { return (unsigned)(size_t)(const LDS_AS char*)p; }
DI bf16x8 ld_frag(const char* lds) { return *(const bf16x8*)lds; }
DI bf16x8 ld_tr2(const char* p0, const char* p1) {
  bf16x4 a = __builtin_amdgcn_ds_read_tr16_b64_v4bf16((LDS_AS bf16x4*)p0);
  bf16x4 b = __builtin_amdgcn_ds_read_tr16_b64_v4bf16((LDS_AS bf16x4*)p1);
  return __builtin_shufflevector(a, b, 0, 1, 2, 3, 4, 5, 6, 7);
}
DI unsigned pack2(float a, float b) {
  f32x2 t = {a, b};
  return __builtin_bit_cast(unsigned, __builtin_convertvector(t, bf16x2));
}
DI float bf_lo(unsigned u) { return __builtin_bit_cast(float, u << 16); }
DI float bf_hi(unsigned u) { return __builtin_bit_cast(float, u & 0xffff0000u); }
DI float xsum4(float x) {
  auto a = __builtin_amdgcn_permlane16_swap(__float_as_uint(x), __float_as_uint(x), false, false);
  x = __uint_as_float(a[0]) + __uint_as_float(a[1]);
  auto b = __builtin_amdgcn_permlane32_swap(__float_as_uint(x), __float_as_uint(x), false, false);
  return __uint_as_float(b[0]) + __uint_as_float(b[1]);
}
DI float xmax4(float x) {
  auto a = __builtin_amdgcn_permlane16_swap(__float_as_uint(x), __float_as_uint(x), false, false);
  x = fmaxf(__uint_as_float(a[0]), __uint_as_float(a[1]));
  auto b = __builtin_amdgcn_permlane32_swap(__float_as_uint(x), __float_as_uint(x), false, false);
  return fmaxf(__uint_as_float(b[0]), __uint_as_float(b[1]));
}

DI float gelu_ln(float z, float k1, float k3, float inv) {
  float t = z * z;
  float w = __builtin_fmaf(t, k3, k1);
  float e = __builtin_amdgcn_exp2f(z * w);
  float d = __builtin_fmaf(e, inv, inv);
  return z * __builtin_amdgcn_rcpf(d);
}
constexpr float GC1 = -2.0f * 0.7978845608028654f * 1.4426950408889634f;
constexpr float GC3 = GC1 * 0.044715f;

DI int swz(int row) { return ((row >> 1) & 1) | ((((row >> 2) ^ (row >> 3)) & 1) << 1); }
DI int src_chunk(int row, int lane) { return ((((lane & 7) >> 1) ^ swz(row)) << 1) | (lane & 1); }

template <int N> DI void wait_v() { asm volatile("s_waitcnt vmcnt(%0)" ::"i"(N) : "memory"); }
#define SB() __builtin_amdgcn_sched_barrier(0)
#define SGB(mask, n) __builtin_amdgcn_sched_group_barrier(mask, n, 0)
DI f32x2 gelu2(f32x2 z, float k1, float k3, float inv) {
  f32x2 t = z * z;
  f32x2 w = __builtin_elementwise_fma(t, f32x2{k3, k3}, f32x2{k1, k1});
  f32x2 u = z * w;
  f32x2 e = {__builtin_amdgcn_exp2f(u[0]), __builtin_amdgcn_exp2f(u[1])};
  f32x2 d = __builtin_elementwise_fma(e, f32x2{inv, inv}, f32x2{inv, inv});
  f32x2 r = {__builtin_amdgcn_rcpf(d[0]), __builtin_amdgcn_rcpf(d[1])};
  return z * r;
}
DI unsigned pack2v(f32x2 t) { return __builtin_bit_cast(unsigned, __builtin_convertvector(t, bf16x2)); }

#define CODE_ADDR(p, sym) do { int off_; asm volatile("s_getpc_b64 %0\n\ts_mov_b32 %1, " #sym "@rel32@lo+4" : "=s"(p), "=s"(off_)); p += off_; } while (0)
constexpr unsigned K2_CODE_BYTES = 29000;

constexpr int K1_A = 114688;
template <bool STATS>
DI void k1_compute(char* smem, const char* xrows, int w, int lane, f32x4 (&acc)[2][8], f32x4& cs, f32x4 (&sum)[2],
                   f32x4 (&sq)[2] STAMP_ARG) {
  const int g = lane >> 4, q = (lane >> 2) & 3, p = lane & 3, r16 = lane & 15;
  const char* sa = smem + K1_A + w * 12288;
  const unsigned la = lds_addr(smem) + K1_A + w * 12288;
  const unsigned xoff = ((32 * w + (lane >> 3)) * D_ + (((lane & 7) ^ (lane >> 3)) << 3)) * 2;
  auto issueA = [&](int st, int slot) {
#pragma unroll
    for (int i = 0; i < 4; ++i) glds16(xrows + st * 128, xoff + i * 8 * D_ * 2, la + slot * 4096 + i * 1024);
  };
  issueA(0, 0); issueA(1, 1); issueA(2, 2);
  const int a0 = r16 * 128 + ((g ^ (r16 & 7)) << 4);
  const int b0 = (8 * g + q) * 128 + (swz(8 * g + q) << 5) + p * 8;
  const int b1 = (8 * g + 4 + q) * 128 + (swz(8 * g + 4 + q) << 5) + p * 8;
  bf16x8 ones;
#pragma unroll
  for (int i = 0; i < 8; ++i) ones[i] = (bf16)1.0f;
  bf16x8 A[2][2], B[2][8], Bw[2];
  auto rdA = [&](const char* abase, const char* bbase, int u) {
    A[u][0] = ld_frag(abase + (a0 ^ (u << 6)));
    A[u][1] = ld_frag(abase + (a0 ^ (u << 6)) + 2048);
    if (STATS) Bw[u] = ld_tr2(bbase + 57344 + (b0 ^ (w << 5)), bbase + 57344 + (b1 ^ (w << 5)));
  };
  auto rdB = [&](const char* bbase, int u, int j) {
    const int o = (j >> 2) * 57344;
    B[u][j] = ld_tr2(bbase + o + (b0 ^ ((j & 3) << 5)), bbase + o + (b1 ^ ((j & 3) << 5)));
  };
  auto kstep = [&](int u, const char* nabase, const char* nbbase, bool more) {
#pragma unroll
    for (int j = 0; j < 8; ++j) {
      acc[0][j] = MFMA16(B[u][j], A[u][0], acc[0][j]);
      acc[1][j] = MFMA16(B[u][j], A[u][1], acc[1][j]);
      if (more) { if (j == 0) rdA(nabase, nbbase, u ^ 1); rdB(nbbase, u ^ 1, j); }
      if (STATS) {
        if (j == 1) sum[0] = MFMA16(ones, A[u][0], sum[0]);
        if (j == 2) sum[1] = MFMA16(ones, A[u][1], sum[1]);
        if (j == 4) sq[0] = MFMA16(A[u][0], A[u][0], sq[0]);
        if (j == 5) sq[1] = MFMA16(A[u][1], A[u][1], sq[1]);
        if (j == 7) cs = MFMA16(Bw[u], ones, cs);
      }
      SB();
    }
  };
  STAMP(1);
  WAIT_V(8);
  BAR(); STAMP(2);
  rdA(sa, smem, 0);
#pragma unroll
  for (int j = 0; j < 8; ++j) rdB(smem, 0, j);
  SB();
  int slot = 0;
#pragma unroll 1
  for (int s = 0; s < 6; ++s) {
    kstep(0, sa + slot * 4096, smem + s * 8192 + 4096, true);
    if (s < 5) WAIT_V(4); else WAIT_V(0);
    asm volatile("s_waitcnt lgkmcnt(14)" ::: "memory");
    __builtin_amdgcn_s_barrier();
    asm volatile("" ::: "memory");
    if (s < 4) issueA(s + 3, slot);
    slot = slot == 2 ? 0 : slot + 1;
    kstep(1, sa + slot * 4096, smem + s * 8192 + 8192, true);
  }
  kstep(0, sa + slot * 4096, smem + 6 * 8192 + 4096, true);
  kstep(1, sa, smem, false);
  STAMP(9);
}

extern "C" __global__ __launch_bounds__(384, 2) void k1_qkv_h(const bf16* __restrict__ x, const bf16* __restrict__ wqkv,
                                                   const bf16* __restrict__ w1, const bf16* __restrict__ w2,
                                                   const bf16* __restrict__ w3, const bf16* __restrict__ w4,
                                                   const bf16* __restrict__ wo, const bf16* __restrict__ gw,
                                                   char* __restrict__ ws) {
  __shared__ __attribute__((aligned(16))) char smem[163840];
  const int tid = threadIdx.x, lane = tid & 63;
  const int w = __builtin_amdgcn_readfirstlane(tid >> 6);
  const int xcd = blockIdx.x & 7, idx = blockIdx.x >> 3;
  STAMP_DECL STAMP(0);
  if (idx >= 28) {
    const int hb = xcd * 4 + (idx - 28);
    float* red = (float*)smem;
    if (tid < 192) ((u32x4*)(ws + OFF_FL))[hb * 192 + tid] = u32x4{0u, 0u, 0u, 0u};
    {
      unsigned sink = 0;
      const char* code;
      CODE_ADDR(code, k2_mega);
      const unsigned off = min((unsigned)(w * 8192 + lane * 128), K2_CODE_BYTES);
      asm volatile("global_load_dword %0, %1, %2" : "+v"(sink) : "v"(off), "s"(code) : "memory");
      WAIT_V(0); touch_keep(sink);
    }
    if (w < 4) {
      if (hb < 28) {
        float a[8] = {};
#pragma unroll
        for (int i = 0; i < 14; ++i) {
          bf16x8 v = *(const bf16x8*)(w3 + (size_t)(w * 112 + i * 8 + (lane >> 3)) * F_ + hb * 64 + (lane & 7) * 8);
#pragma unroll
          for (int e = 0; e < 8; ++e) a[e] += (float)v[e];
        }
#pragma unroll
        for (int e = 0; e < 8; ++e) {
          a[e] += __shfl_xor(a[e], 8);
          a[e] = xsum4(a[e]);
        }
        if (lane < 8) {
#pragma unroll
          for (int e = 0; e < 8; ++e) red[w * 64 + lane * 8 + e] = a[e];
        }
      }
    } else {
      bf16x8 gv = {};
      if (lane < 56) gv = *(const bf16x8*)(gw + lane * 8);
      float* G = (float*)(ws + OFF_G);
#pragma unroll 1
      for (int rb = 0; rb < 2; ++rb) {
        bf16x8 xv[16];
        const int row0 = hb * 64 + (w - 4) * 32 + rb * 16;
#pragma unroll
        for (int r = 0; r < 16; ++r) {
          xv[r] = bf16x8{};
          if (lane < 56) xv[r] = *(const bf16x8*)(x + (row0 + r) * D_ + lane * 8);
        }
        float mine = 0.f;
#pragma unroll
        for (int r = 0; r < 16; ++r) {
          float s = 0.f;
#pragma unroll
          for (int i = 0; i < 4; ++i) {
            bf16x2 a = {xv[r][2 * i], xv[r][2 * i + 1]}, b = {gv[2 * i], gv[2 * i + 1]};
            s = __builtin_amdgcn_fdot2_f32_bf16(a, b, s, false);
          }
#pragma unroll
          for (int o = 8; o; o >>= 1) s += __shfl_xor(s, o);
          s = xsum4(s);
          mine = lane == r ? s : mine;
        }
        if (lane < 16) {
          f32x2 gg = {1.f / (1.f + __expf(-mine)), 1.f / (1.f + __expf(mine))};
          *(f32x2*)(G + (row0 + lane) * 2) = gg;
        }
      }
    }
    BAR();
    if (hb < 28 && tid < 64) ((float*)(ws + OFF_CS))[hb * 64 + tid] = (red[tid] + red[64 + tid]) + (red[128 + tid] + red[192 + tid]);
    STAMP_FLUSH(ws, 0);
    return;
  }
  const int panel = 4 * (xcd >> 1) + (idx & 3), ct = 7 * (xcd & 1) + (idx >> 2);
  const bool mixed = ct < 7;
  const int u0 = mixed ? ct : 2 * ct - 7;
  if (w >= 4) {
    const int r = lane >> 3;
    const bool second = w == 5;
    const unsigned l0 = lds_addr(smem) + (second ? 57344 : 0);
    const char* base = (second && mixed) ? (const char*)(w1 + ct * 64) : (const char*)(wqkv + (u0 + (second ? 1 : 0)) * 64);
    const int ld = (second && mixed) ? 896 : 2688;
    const unsigned cE = src_chunk(r, lane) * 16, cO = src_chunk(r + 8, lane) * 16;
    auto issue = [&](int st) {
#pragma unroll
      for (int i = 0; i < 8; ++i) glds16(base + (size_t)st * 64 * ld, (8 * i + r) * ld + ((i & 1) ? cO : cE), l0 + st * 8192 + i * 1024);
    };
    unsigned sink = 0;
    {
      const char* code;
      CODE_ADDR(code, k1_qkv_h);
      touch8k(sink, code, w - 4, lane);
    }
    touch8k(sink, (const char*)(x + (size_t)panel * 128 * D_), (idx >> 2) * 2 + (w - 4), lane);
#pragma unroll 1
    for (int st = 0; st < 7; ++st) {
      if (st == 2) { WAIT_V(8); BAR(); }
      if (st == 5) { WAIT_V(24); BAR(); }
      issue(st);
    }
    WAIT_V(32); BAR();
    WAIT_V(24); BAR();
    WAIT_V(16); BAR();
    __builtin_amdgcn_s_sleep(4);
#pragma unroll
    for (int m = 0; m < 5; ++m) {
      const int c = idx * 2 + (w - 4) + 56 * m;
      touch8k(sink, c < 49 ? (const char*)w2 : (const char*)w3, c < 49 ? c : c < 245 ? c - 49 : 0, lane);
    }
    WAIT_V(13); BAR();
    WAIT_V(5); BAR();
    if (mixed) BAR();
    WAIT_V(0);
    touch_keep(sink);
    return;
  }
  const int g = lane >> 4, r16 = lane & 15;
  f32x4 acc[2][8] = {};
  f32x4 cs = {};
  f32x4 sum[2] = {}, sq[2] = {};
  float* ex = (float*)smem;
  const char* xrows = (const char*)(x + (size_t)panel * 128 * D_);
  if (mixed) {
    k1_compute<true>(smem, xrows, w, lane, acc, cs, sum, sq STAMP_PASS);
    if (r16 == 0) *(f32x4*)(ex + w * 16 + 4 * g) = cs;
  } else {
    k1_compute<false>(smem, xrows, w, lane, acc, cs, sum, sq STAMP_PASS);
  }
  {
    const int gl = (g >> 1) | ((g & 1) << 1);
#pragma unroll
    for (int jj = 0; jj < 4; ++jj) {
      if (jj < 2 || !mixed) {
#pragma unroll
        for (int rf = 0; rf < 2; ++rf) {
          const int tt = panel * 8 + 2 * w + rf;
          unsigned a0 = pack2(acc[rf][2 * jj][0], acc[rf][2 * jj][1]), a1 = pack2(acc[rf][2 * jj][2], acc[rf][2 * jj][3]);
          unsigned b0 = pack2(acc[rf][2 * jj + 1][0], acc[rf][2 * jj + 1][1]), b1 = pack2(acc[rf][2 * jj + 1][2], acc[rf][2 * jj + 1][3]);
          auto s0 = __builtin_amdgcn_permlane16_swap(a0, b0, false, false);
          auto s1 = __builtin_amdgcn_permlane16_swap(a1, b1, false, false);
          u32x4 v = {s0[0], s1[0], s0[1], s1[1]};
          *(u32x4*)(ws + OFF_QKV + ((size_t)((tt * 168 + u0 * 8 + 4 * jj + gl) * 16 + r16) << 4)) = v;
        }
      }
    }
  }
  if (!mixed) {
    unsigned sink = 0;
    const int c = ((xcd >> 1) * 28 + idx) * 4 + w;
    touch8k(sink, c < 49 ? (const char*)wo : (const char*)w4, c < 49 ? c : c < 245 ? c - 49 : 0, lane);
    WAIT_V(0); touch_keep(sink);
  } else {
    BAR();
    const int nb = ct * 64;
    f32x4 c4[4];
#pragma unroll
    for (int j = 0; j < 4; ++j) c4[j] = *(const f32x4*)(ex + j * 16 + 4 * g);
    unsigned hv[2][4][2];
#pragma unroll
    for (int rf = 0; rf < 2; ++rf) {
      float mean = sum[rf][0] * (1.f / D_);
      float dg = (r16 & 2) ? ((r16 & 1) ? sq[rf][3] : sq[rf][2]) : ((r16 & 1) ? sq[rf][1] : sq[rf][0]);
      dg = xsum4(g == (r16 >> 2) ? dg : 0.f);
      float var = fmaxf(dg * (1.f / D_) - mean * mean, 0.f);
      float a = __builtin_amdgcn_rsqf(var + 1e-5f);
      float k1 = GC1 * a, k3 = GC3 * a * a * a, inv = __builtin_amdgcn_rcpf(a);
      const f32x2 nm = {-mean, -mean};
#pragma unroll
      for (int j = 0; j < 4; ++j) {
        f32x2 z0 = __builtin_elementwise_fma(nm, f32x2{c4[j][0], c4[j][1]}, f32x2{acc[rf][4 + j][0], acc[rf][4 + j][1]});
        f32x2 z1 = __builtin_elementwise_fma(nm, f32x2{c4[j][2], c4[j][3]}, f32x2{acc[rf][4 + j][2], acc[rf][4 + j][3]});
        hv[rf][j][0] = pack2v(gelu2(z0, k1, k3, inv));
        hv[rf][j][1] = pack2v(gelu2(z1, k1, k3, inv));
      }
    }
#pragma unroll
    for (int rf = 0; rf < 2; ++rf)
#pragma unroll
      for (int j = 0; j < 4; ++j) asm volatile("" : "+v"(hv[rf][j][0]), "+v"(hv[rf][j][1]));
#pragma unroll
    for (int jj = 0; jj < 2; ++jj) {
      const int ks = (nb >> 5) + jj;
      int sh[2];
#pragma unroll
      for (int h = 0; h < 2; ++h) {
        const int grp = (nb + 32 * jj + 16 * h) / 112;
        sh[h] = (grp & 2 ? 2 : 1) * (grp & 1 ? -1 : 1);
      }
#pragma unroll
      for (int rf = 0; rf < 2; ++rf) {
        const int tok = panel * 128 + 32 * w + 16 * rf + r16;
        int t2[2];
        u32x2 v[2];
#pragma unroll
        for (int h = 0; h < 2; ++h) {
          t2[h] = tok + sh[h];
          const bool ok = (unsigned)t2[h] < (unsigned)S_;
          t2[h] &= S_ - 1;
          v[h][0] = ok ? hv[rf][2 * jj + h][0] : 0u;
          v[h][1] = ok ? hv[rf][2 * jj + h][1] : 0u;
        }
        char* d0 = ws + OFF_HS + ((size_t)(((t2[0] >> 4) * 14 + ks) * 64 + g * 16 + (t2[0] & 15)) << 4);
        if (ks != 3 && ks != 10) {
          u32x4 vv = {v[0][0], v[0][1], v[1][0], v[1][1]};
          *(u32x4*)d0 = vv;
        } else {
          char* d1 = ws + OFF_HS + ((size_t)(((t2[1] >> 4) * 14 + ks) * 64 + g * 16 + (t2[1] & 15)) << 4) + 8;
          *(u32x2*)d0 = v[0];
          *(u32x2*)d1 = v[1];
        }
      }
    }
  }
  STAMP(10);
  STAMP_FLUSH(ws, 0);
}

typedef __attribute__((address_space(1))) unsigned gu32;
typedef __amdgpu_buffer_rsrc_t rsrc_t;
DI u32x4 ld16_sc1(rsrc_t r, unsigned off) { return __builtin_amdgcn_raw_buffer_load_b128(r, off, 0, 16); }
DI u32x4 ld16(rsrc_t r, unsigned off) { return __builtin_amdgcn_raw_buffer_load_b128(r, off, 0, 0); }
DI void st16_sc1(rsrc_t r, unsigned off, u32x4 v) { __builtin_amdgcn_raw_buffer_store_b128(v, r, off, 0, 16); }
DI void st8_sc1(rsrc_t r, unsigned off, u32x2 v) { __builtin_amdgcn_raw_buffer_store_b64(v, r, off, 0, 16); }
DI bf16x8 as_frag(u32x4 v) { return __builtin_bit_cast(bf16x8, v); }
DI void post_flags(char* ws, int kind, int consumer, int word, int lane) {
  if (lane < 14)
    __hip_atomic_store((gu32*)(ws + OFF_FL) + (kind * 256 + consumer) * 32 + word, 1u, __ATOMIC_RELAXED, __HIP_MEMORY_SCOPE_AGENT);
}
DI void wait_flags(char* ws, int kind, int nwords, int lane) {
  gu32* f = (gu32*)(ws + OFF_FL) + (kind * 256 + blockIdx.x) * 32 + (lane < nwords ? lane : 0);
#pragma unroll 1
  for (int spin = 0; spin < (1 << 18); ++spin) {
    unsigned v = __hip_atomic_load(f, __ATOMIC_RELAXED, __HIP_MEMORY_SCOPE_AGENT);
    if (__all(v == 1u)) break;
    __builtin_amdgcn_s_sleep(2);
  }
  asm volatile("" ::: "memory");
}
constexpr int L_H1 = 57344, L_X = 102400, L_EV = 110592, L_C = 114688;
DI void ev_signal(char* smem, int ev, int wq, int lane) {
  asm volatile("s_waitcnt lgkmcnt(0)" ::: "memory");
  if (lane == 0) *(volatile LDS_AS unsigned*)(LDS_AS char*)(smem + L_EV + ev * 16 + wq * 4) = 1u;
  asm volatile("" ::: "memory");
}
DI void ev_wait(char* smem, int ev) {
  asm volatile("" ::: "memory");
#pragma unroll 1
  for (int spin = 0; spin < (1 << 22); ++spin) {
    u32x4 v = *(volatile LDS_AS u32x4*)(LDS_AS char*)(smem + L_EV + ev * 16);
    if (__builtin_amdgcn_readfirstlane(v[0] & v[1] & v[2] & v[3])) break;
    __builtin_amdgcn_s_sleep(1);
  }
  asm volatile("" ::: "memory");
}
DI void ev_signal_all(char* smem, int ev, int lane) {
  asm volatile("" ::: "memory");
  if (lane < 4) *(volatile LDS_AS unsigned*)(LDS_AS char*)(smem + L_EV + ev * 16 + lane * 4) = 1u;
  asm volatile("" ::: "memory");
}
DI int slotA(int c, int m) {
  if (m < 4) { const int n = 4 * c + m; return n < 8 ? n * 4096 : L_H1 + (n - 8) * 4096; }
  const int n = 3 * c + m - 4;
  return n < 6 ? 32768 + n * 4096 : n < 11 ? 90112 + (n - 6) * 4096 : L_C;
}

extern "C" __global__ __launch_bounds__(512, 2) void k2_mega(const bf16* __restrict__ x, const bf16* __restrict__ w2,
                                                  const bf16* __restrict__ w3, const bf16* __restrict__ w4,
                                                  const bf16* __restrict__ wo, char* __restrict__ ws,
                                                  bf16* __restrict__ out) {
  __shared__ __attribute__((aligned(16))) char smem[163840];
  const int tid = threadIdx.x, lane = tid & 63;
  const int w8 = __builtin_amdgcn_readfirstlane(tid >> 6);
  const int grp = w8 >> 2, w = w8 & 3;
  const int xcd = blockIdx.x & 7, idx = blockIdx.x >> 3;
  STAMP_DECL STAMP(0);
  const unsigned l0 = lds_addr(smem);
  const bool has_gemm = idx < 28;
  const int rt64 = 4 * xcd + (idx & 3), c7 = idx >> 2;
  const int panel = 2 * xcd + (idx & 1), c14 = idx >> 1;
  const int head = c14;
  const int g = lane >> 4, q = (lane >> 2) & 3, p = lane & 3, r16 = lane & 15, r8 = lane >> 3;
  const rsrc_t rs = __builtin_amdgcn_make_buffer_rsrc(ws, 0, (int)WS_USED, 0x00020000);
  const int btr = (4 * g + q) * 128 + (swz(4 * g + q) << 5) + p * 8;
  const unsigned cE = src_chunk(r8, lane) * 16, cO = src_chunk(r8 + 8, lane) * 16;
  auto loadW = [&](const char* base, int ld, int g0, int n, unsigned dst) {
    const unsigned oE = r8 * ld + cE, oO = (r8 + 8) * ld + cO;
#pragma unroll 1
    for (int m = g0; m < g0 + n; ++m) {
      glds16(base + (size_t)m * 16 * ld, oE, dst + m * 2048);
      glds16(base + (size_t)m * 16 * ld, oO, dst + m * 2048 + 1024);
    }
  };
  const char* w3b = (const char*)(w3 + c14 * 128);
  const char* wob = (const char*)(wo + c7 * 64);
  const char* w4b = (const char*)(w4 + c7 * 64);
  if (tid < 64) ((unsigned*)(smem + L_EV))[tid] = 0u;
  const int cons3 = ((2 * lane + ((rt64 >> 1) & 1)) << 3) | xcd;
  const int cons4 = ((4 * (lane >> 1) + 2 * (panel & 1) + (lane & 1)) << 3) | xcd;

  if (grp == 0) {
    if (!has_gemm) { BAR(); return; }
    __builtin_amdgcn_s_setprio(2);
    loadW((const char*)(w2 + c7 * 64), D_ * 2, 7 * w, 7, l0);
    {
      const char* bx = (const char*)(x + (size_t)(rt64 * 64 + 16 * w) * D_ + c7 * 64);
#pragma unroll
      for (int i = 0; i < 2; ++i) glds16(bx, ((8 * i + r8) * D_ + (((lane & 7) ^ r8) << 3)) * 2, l0 + L_X + w * 2048 + i * 1024);
    }
    u32x4 ha[14];
#pragma unroll
    for (int ks = 0; ks < 14; ++ks) ha[ks] = ld16(rs, OFF_HS + (((rt64 * 4 + w) * 14 + ks) * 64 + lane) * 16);
    BAR();
    WAIT_V(14);
    ev_signal(smem, 0, w, lane);
    ev_wait(smem, 0);
    STAMP(1);
    {
      f32x4 acc[4] = {};
      bf16x8 bq[3][4];
      auto rdB = [&](int ks, int j) {
        const char* bp = smem + ks * 4096 + (btr ^ (j << 5));
        bq[ks % 3][j] = ld_tr2(bp, bp + 2048);
      };
#pragma unroll
      for (int j = 0; j < 4; ++j) { rdB(0, j); rdB(1, j); }
      const int trow = 16 * w + r16;
      u32x2 xr[4];
      SB();
#pragma unroll
      for (int ks = 0; ks < 14; ++ks) {
#pragma unroll
        for (int j = 0; j < 4; ++j) {
          acc[j] = MFMA16(bq[ks % 3][j], as_frag(ha[ks]), acc[j]);
          if (ks + 2 < 14) rdB(ks + 2, j);
          if (ks == 12) xr[j] = *(const u32x2*)(smem + L_X + trow * 128 + (((2 * j + (g >> 1)) ^ (trow & 7)) << 4) + (g & 1) * 8);
          SB();
        }
      }
      STAMP(11);
      float sum = 0.f, sq = 0.f;
      u32x4 x1k[2];
#pragma unroll
      for (int j = 0; j < 4; ++j) {
        unsigned lo = pack2(acc[j][0] + bf_lo(xr[j][0]), acc[j][1] + bf_hi(xr[j][0]));
        unsigned hi = pack2(acc[j][2] + bf_lo(xr[j][1]), acc[j][3] + bf_hi(xr[j][1]));
        x1k[j >> 1][2 * (j & 1)] = lo; x1k[j >> 1][2 * (j & 1) + 1] = hi;
        float e0 = bf_lo(lo), e1 = bf_hi(lo), e2 = bf_lo(hi), e3 = bf_hi(hi);
        sum += (e0 + e1) + (e2 + e3);
        sq += (e0 * e0 + e1 * e1) + (e2 * e2 + e3 * e3);
      }
#pragma unroll
      for (int jj = 0; jj < 2; ++jj) st16_sc1(rs, OFF_X1 + (((rt64 * 4 + w) * 14 + 2 * c7 + jj) * 64 + lane) * 16, x1k[jj]);
      u32x2 t = {__float_as_uint(xsum4(sum)), __float_as_uint(xsum4(sq))};
      if (g == 0) st8_sc1(rs, OFF_ST + (rt64 * 4 + w) * 1024 + (c7 >> 1) * 256 + r16 * 16 + (c7 & 1) * 8, t);
    }
    WAIT_V(0);
    ev_signal(smem, 8, w, lane);
    ev_wait(smem, 8);
    if (w == 0) post_flags(ws, 0, cons3, (rt64 & 1) * 7 + c7, lane);
    STAMP(2);
    loadW(w3b, F_ * 2, 7 * w, 7, l0);
    loadW(w3b + 128, F_ * 2, 22 + w, 1, l0 + L_H1);
    WAIT_V(0);
    STAMP(3);
    ev_signal(smem, 2, w, lane);
    if (w == 0) {
      wait_flags(ws, 0, 14, lane);
      ev_signal_all(smem, 9, lane);
    } else {
      ev_wait(smem, 9);
    }
    STAMP(4);
    {
      u32x4 a[2][14];
      const unsigned aoff = OFF_X1 + ((panel * 8 + 2 * w) * 14 * 64 + lane) * 16;
      auto ldA = [&](int ks) {
        a[0][ks] = ld16_sc1(rs, aoff + ks * 1024);
        a[1][ks] = ld16_sc1(rs, aoff + 14336 + ks * 1024);
      };
#pragma unroll
      for (int ks = 0; ks < 6; ++ks) ldA(ks);
      ev_wait(smem, 2);
      ev_wait(smem, 6);
      STAMP(5);
      u32x4 stv[2];
      float g1[2];
      f32x4 c4[8];
      f32x4 acc[2][8] = {};
      bf16x8 bq[8];
      auto rdB = [&](int n) {
        const int ks = n >> 3, j = n & 7;
        const int base = j < 4 ? ks * 4096 : (ks < 13 ? L_H1 + ks * 4096 : L_C);
        const char* bp = smem + base + (btr ^ ((j & 3) << 5));
        return ld_tr2(bp, bp + 2048);
      };
#pragma unroll
      for (int n = 0; n < 6; ++n) bq[n] = rdB(n);
      SB();
#pragma unroll
      for (int ks = 0; ks < 14; ++ks) {
        if (ks == 12) ev_wait(smem, 7);
        if (ks == 9 && lane == 0) *(volatile LDS_AS unsigned*)(LDS_AS char*)(smem + L_EV + 15 * 16 + w * 4) = 1u;
#pragma unroll
        for (int j = 0; j < 8; ++j) {
          const int n = ks * 8 + j;
          acc[0][j] = MFMA16(bq[j], as_frag(a[0][ks]), acc[0][j]);
          acc[1][j] = MFMA16(bq[j], as_frag(a[1][ks]), acc[1][j]);
          if (n + 6 < 112) bq[(j + 6) & 7] = rdB(n + 6);
          if (j == 1 && ks + 6 < 14) ldA(ks + 6);
          if (ks == 8 && j < 2) {
            stv[j] = ld16_sc1(rs, OFF_ST + (panel * 8 + 2 * w + j) * 1024 + lane * 16);
            g1[j] = ((const float*)(ws + OFF_G))[(panel * 128 + 32 * w + 16 * j + r16) * 2 + 1];
          }
          if (ks == 9) c4[j] = *(const f32x4*)(ws + OFF_CS + (size_t)(c14 * 128 + 16 * j + 4 * g) * 4);
          SB();
        }
      }
      ev_signal(smem, 3, w, lane);
      STAMP(6);
#pragma unroll
      for (int rf = 0; rf < 2; ++rf) {
        float s1 = __uint_as_float(stv[rf][0]), s2 = __uint_as_float(stv[rf][1]);
        if (g < 3) { s1 += __uint_as_float(stv[rf][2]); s2 += __uint_as_float(stv[rf][3]); }
        s1 = xsum4(s1); s2 = xsum4(s2);
        float mean = s1 * (1.f / D_);
        float var = fmaxf(s2 * (1.f / D_) - mean * mean, 0.f);
        float a_ = __builtin_amdgcn_rsqf(var + 1e-5f);
        float k1 = GC1 * a_, k3 = GC3 * a_ * a_ * a_, inv = __builtin_amdgcn_rcpf(fmaxf(a_ * g1[rf], 1e-30f));
        const f32x2 nm = {-mean, -mean};
#pragma unroll
        for (int jj = 0; jj < 4; ++jj) {
          u32x4 v;
#pragma unroll
          for (int h = 0; h < 2; ++h) {
            const int j = 2 * jj + h;
            f32x2 z0 = __builtin_elementwise_fma(nm, f32x2{c4[j][0], c4[j][1]}, f32x2{acc[rf][j][0], acc[rf][j][1]});
            f32x2 z1 = __builtin_elementwise_fma(nm, f32x2{c4[j][2], c4[j][3]}, f32x2{acc[rf][j][2], acc[rf][j][3]});
            v[2 * h] = pack2v(gelu2(z0, k1, k3, inv));
            v[2 * h + 1] = pack2v(gelu2(z1, k1, k3, inv));
          }
          st16_sc1(rs, OFF_AC + (((panel * 8 + 2 * w + rf) * 72 + 16 + c14 * 4 + jj) * 64 + lane) * 16, v);
        }
      }
    }
    STAMP(7);
    WAIT_V(0);
    STAMP(12);
    ev_signal(smem, 10, w, lane);
    if (w == 0) {
      ev_wait(smem, 10);
      post_flags(ws, 2, cons4, c14, lane);
    }
    __builtin_amdgcn_s_setprio(0);
  } else {
    const int gb = (7 * head) >> 1;
    const int qw = panel * 128 + 32 * w;
    BAR();
    {
      const unsigned lv = (lane & 48) | ((lane & 15) ^ ((lane & 16) >> 1));
      const char* src = ws + OFF_QKV + (size_t)(56 + gb) * 256;
#pragma unroll
      for (int m = 0; m < 6; ++m) {
        const int lt = 6 * w + m;
        int kt = panel * 8 - 16 + lt;
        kt = kt < 0 ? 0 : kt;
        glds16(src + (size_t)kt * 43008, lane * 16, l0 + L_C + lt * 1024);
        glds16(src + (size_t)kt * 43008 + 56 * 256, lv * 16, l0 + L_C + 24576 + lt * 1024);
      }
    }
    u32x4 qv[2];
#pragma unroll
    for (int h = 0; h < 2; ++h)
      asm volatile("global_load_dwordx4 %0, %1, %2" : "=v"(qv[h]) : "v"((((panel * 8 + 2 * w + h) * 168 + gb) * 16 + lane) * 16), "s"(ws + OFF_QKV) : "memory");
    if (has_gemm) {
      const unsigned o0 = (88 * w + r8) * (F_ * 2) + ((w & 1) ? cO : cE), o1 = (88 * w + r8) * (F_ * 2) + ((w & 1) ? cE : cO);
      const unsigned ld = l0 + L_H1 + w * 11264;
#pragma unroll
      for (int m = 0; m < 11; ++m) glds16(w3b + 128, ((m & 1) ? o1 : o0) + m * 8 * F_ * 2, ld + m * 1024);
      WAIT_V(11);
    } else {
      WAIT_V(0);
    }
    asm volatile("" : "+v"(qv[0]), "+v"(qv[1])::"memory");
    ev_signal(smem, 4, w, lane);
    ev_wait(smem, 4);
    STAMP(1);
    const char* sK = smem + L_C;
    const char* sV = smem + L_C + 24576;
    const bool odd = head & 1;
    const bool z01 = odd && g == 0, z23 = !odd && g == 3;
    bf16x8 qf[2];
#pragma unroll
    for (int h = 0; h < 2; ++h) {
      qv[h][0] = z01 ? 0u : qv[h][0]; qv[h][1] = z01 ? 0u : qv[h][1];
      qv[h][2] = z23 ? 0u : qv[h][2]; qv[h][3] = z23 ? 0u : qv[h][3];
      qf[h] = as_frag(qv[h]);
    }
    const float NEG = -__builtin_inff();
    constexpr float SC = 0.1889822365046136f * 1.4426950408889634f;
    f32x4 s[2][17];
    float mx[2] = {NEG, NEG};
    auto max4 = [&](float m, const f32x4& v) {
      return __builtin_fmaxf(__builtin_fmaxf(__builtin_fmaxf(__builtin_fmaxf(m, v[0]), v[1]), v[2]), v[3]);
    };
    auto finish = [&](int h, int t) {
      if (t == 0 || t == 16) {
#pragma unroll
        for (int i = 0; i < 4; ++i) {
          const int b = 4 * g + i;
          s[h][t][i] = ((b > r16) == (t == 0)) ? s[h][t][i] : NEG;
        }
      }
      mx[h] = max4(mx[h], s[h][t]);
    };
    {
      bf16x8 kf[6];
#pragma unroll
      for (int t = 0; t < 6; ++t) kf[t] = ld_frag(sK + (2 * w + t) * 1024 + lane * 16);
      SB();
#pragma unroll
      for (int t = 0; t < 20; ++t) {
        if (t < 18) {
          f32x4 z = {};
          if (t < 17) s[0][t] = MFMA16(kf[t % 6], qf[0], z);
          if (t > 0) s[1][t - 1] = MFMA16(kf[t % 6], qf[1], z);
          if (t + 6 < 18) kf[t % 6] = ld_frag(sK + (2 * w + t + 6) * 1024 + lane * 16);
        }
        if (t >= 2) {
          if (t - 2 < 17) finish(0, t - 2);
          if (t - 2 > 0) finish(1, t - 3);
        }
        SB();
      }
    }
    if (qw < 256) {
#pragma unroll
      for (int h = 0; h < 2; ++h) {
        const int tstart = 16 - (qw >> 4) - h;
        mx[h] = NEG;
#pragma unroll
        for (int t = 0; t < 17; ++t) {
          if (t < tstart) s[h][t] = f32x4{NEG, NEG, NEG, NEG};
          mx[h] = max4(mx[h], s[h][t]);
        }
      }
    }
    const float mc[2] = {-xmax4(mx[0]) * SC, -xmax4(mx[1]) * SC};
    STAMP(2);
    WAIT_V(0);
    ev_signal(smem, 6, w, lane);
    float gt[2];
#pragma unroll
    for (int h = 0; h < 2; ++h) gt[h] = ((const float*)(ws + OFF_G))[(qw + 16 * h + r16) * 2];
    STAMP(3);
    f32x4 o[2][2] = {}, lsum[2] = {};
    bf16x8 ones;
#pragma unroll
    for (int i = 0; i < 8; ++i) ones[i] = (bf16)1.0f;
    auto pexp = [&](const f32x4& sv, float m, unsigned& lo, unsigned& hi) {
      f32x2 a = __builtin_elementwise_fma(f32x2{sv[0], sv[1]}, f32x2{SC, SC}, f32x2{m, m});
      f32x2 b = __builtin_elementwise_fma(f32x2{sv[2], sv[3]}, f32x2{SC, SC}, f32x2{m, m});
      lo = pack2(__builtin_amdgcn_exp2f(a[0]), __builtin_amdgcn_exp2f(a[1]));
      hi = pack2(__builtin_amdgcn_exp2f(b[0]), __builtin_amdgcn_exp2f(b[1]));
    };
    const int voff0 = (p >> 1) * 256 + (((4 * g + q) ^ ((p >> 1) << 3)) << 4) + (p & 1) * 8;
    bf16x8 vf[2][2], pf[2][2];
#pragma unroll
    for (int kk = 0; kk < 10; ++kk) {
      if (kk < 9) {
        const char* vb = sV + (2 * w + 2 * kk) * 1024 + voff0;
        vf[kk & 1][0] = ld_tr2(vb, vb + 1024);
        vf[kk & 1][1] = ld_tr2(vb + 512, vb + 1536);
#pragma unroll
        for (int h = 0; h < 2; ++h) {
          unsigned p0 = 0, p1 = 0, p2 = 0, p3 = 0;
          const int t0 = 2 * kk - h, t1 = t0 + 1;
          if (t0 >= 0) pexp(s[h][t0 < 0 ? 0 : t0], mc[h], p0, p1);
          if (t1 <= 16) pexp(s[h][t1 > 16 ? 16 : t1], mc[h], p2, p3);
          pf[kk & 1][h] = as_frag(u32x4{p0, p1, p2, p3});
        }
      }
      if (kk > 0) {
        const int c = (kk - 1) & 1;
#pragma unroll
        for (int h = 0; h < 2; ++h) {
          o[h][0] = MFMA16(vf[c][0], pf[c][h], o[h][0]);
          o[h][1] = MFMA16(vf[c][1], pf[c][h], o[h][1]);
          lsum[h] = MFMA16(ones, pf[c][h], lsum[h]);
        }
      }
      SGB(0x100, 4);
#pragma unroll
      for (int m = 0; m < 6; ++m) { SGB(0x8, 1); SGB(0x402, 5); }
      SB();
    }
    ev_signal(smem, 5, w, lane);
    STAMP(4);
#pragma unroll
    for (int h = 0; h < 2; ++h) {
      float sc = gt[h] * __builtin_amdgcn_rcpf(lsum[h][0]);
      u32x4 v;
      v[0] = z01 ? 0u : pack2(o[h][0][0] * sc, o[h][0][1] * sc);
      v[1] = z01 ? 0u : pack2(o[h][0][2] * sc, o[h][0][3] * sc);
      v[2] = z23 ? 0u : pack2(o[h][1][0] * sc, o[h][1][1] * sc);
      v[3] = z23 ? 0u : pack2(o[h][1][2] * sc, o[h][1][3] * sc);
      st16_sc1(rs, OFF_AC + (((panel * 8 + 2 * w + h) * 72 + head) * 64 + lane) * 16, v);
    }
    if (!has_gemm) {
      WAIT_V(0);
      ev_signal(smem, 11, w, lane);
      if (w == 0) {
        ev_wait(smem, 11);
        post_flags(ws, 1, cons4, head, lane);
      }
      return;
    }
    ev_wait(smem, 5);
    glds16(w3b + 128 + (size_t)(416 + 8 * w) * F_ * 2, r8 * (F_ * 2) + ((w & 1) ? cO : cE), l0 + L_C + w * 1024);
  }
  {
    unsigned aoff[4];
#pragma unroll
    for (int rt = 0; rt < 4; ++rt) aoff[rt] = OFF_AC + (((rt64 * 4 + (rt ^ (w8 >> 1))) * 72 + w) * 64 + lane) * 16;
    const int bx = btr ^ ((w8 & 1) << 6);
    f32x4 acc[4][4] = {};
    u32x4 a[3][4];
    bf16x8 bp[7][4];
    auto ldA = [&](int buf, int k4) {
#pragma unroll
      for (int rt = 0; rt < 4; ++rt) a[buf][rt] = ld16_sc1(rs, aoff[rt] + k4 * 4096);
    };
    auto rdB = [&](bf16x8 (&bf)[4], const char* base) {
#pragma unroll
      for (int j = 0; j < 4; ++j) bf[j] = ld_tr2(base + (bx ^ (j << 5)), base + (bx ^ (j << 5)) + 2048);
    };
    auto mma = [&](const u32x4 (&af)[4], const bf16x8 (&bf)[4]) {
#pragma unroll
      for (int rt = 0; rt < 4; ++rt)
#pragma unroll
        for (int j = 0; j < 4; ++j) acc[rt][j] = MFMA16(bf[j], as_frag(af[rt]), acc[rt][j]);
    };
    unsigned v4[4];
#pragma unroll
    for (int i = 0; i < 4; ++i) v4[i] = (32 * w + 8 * i + r8) * (D_ * 2) + ((i & 1) ? cO : cE);
    auto loadA4 = [&](int m) {
      const char* src = w4b + (size_t)(28 * 32 + m * 128) * D_ * 2;
      const unsigned ld = l0 + slotA(w, m);
      glds16c<0>(src, v4[0], ld); glds16c<1024>(src, v4[1], ld); glds16c<2048>(src, v4[2], ld); glds16c<3072>(src, v4[3], ld);
    };
    if (grp == 1) {
      unsigned vo[4];
#pragma unroll
      for (int i = 0; i < 4; ++i) {
        int d = 8 * i + r8 - 4 * (w & 1);
        d = d < 0 ? 0 : (d > 27 ? 27 : d);
        vo[i] = (28 * w + d) * (D_ * 2) + ((i & 1) ? cO : cE);
      }
      const unsigned lst = l0 + L_C + 4096 + w * 11264;
      const char* sst = smem + L_C + 4096 + w * 11264;
      auto issue = [&](int n) {
        const bool o_ = n >= 6 && n < 10;
        const char* src = o_ ? wob + (size_t)(n - 6) * 112 * D_ * 2 : w4b + (size_t)(n < 6 ? n : 6) * 128 * D_ * 2;
        if (n & 1) {
          glds16c<4096>(src, o_ ? vo[0] : v4[0], lst); glds16c<5120>(src, o_ ? vo[1] : v4[1], lst);
          glds16c<6144>(src, o_ ? vo[2] : v4[2], lst); glds16c<7168>(src, o_ ? vo[3] : v4[3], lst);
        } else {
          glds16c<0>(src, o_ ? vo[0] : v4[0], lst); glds16c<1024>(src, o_ ? vo[1] : v4[1], lst);
          glds16c<2048>(src, o_ ? vo[2] : v4[2], lst); glds16c<3072>(src, o_ ? vo[3] : v4[3], lst);
        }
      };
      issue(0); issue(1);
      WAIT_V(8);
      ev_signal(smem, 7, w, lane);
      STAMP(5);
      if (w == 0) {
        ev_wait(smem, 7);
        post_flags(ws, 1, cons4, head, lane);
        WAIT_V(0);
      }
#pragma unroll
      for (int n = 0; n < 6; ++n) {
        WAIT_V(4);
        rdB(bp[n], sst + (n & 1) * 4096);
        asm volatile("s_waitcnt lgkmcnt(0)" ::: "memory");
        issue(n + 2);
      }
      STAMP(6);
      ev_wait(smem, 15);
#pragma unroll
      for (int m = 0; m < 4; ++m) loadA4(m);
      if (w == 0) {
        wait_flags(ws, 1, 16, lane);
        ev_signal_all(smem, 12, lane);
      } else {
        ev_wait(smem, 12);
      }
      STAMP(7);
      ldA(0, 0); ldA(1, 1);
      WAIT_V(8);
      ev_signal(smem, 14, w, lane);
      rdB(bp[6], sst);
      asm volatile("s_waitcnt lgkmcnt(0)" ::: "memory");
      WAIT_V(0);
      issue(8);
      mma(a[0], bp[6]);
      SB();
      rdB(bp[6], sst + 4096);
      asm volatile("s_waitcnt lgkmcnt(0)" ::: "memory");
      issue(9);
      mma(a[1], bp[6]);
      SB();
      ldA(0, 2); ldA(1, 3);
      WAIT_V(8);
      rdB(bp[6], sst);
      asm volatile("s_waitcnt lgkmcnt(0)" ::: "memory");
      WAIT_V(0);
      issue(10);
      mma(a[0], bp[6]);
      SB();
      rdB(bp[6], sst + 4096);
      mma(a[1], bp[6]);
      SB();
      STAMP(8);
      WAIT_V(0);
      rdB(bp[6], sst);
      STAMP(12);
    } else {
      ev_wait(smem, 3);
#pragma unroll
      for (int m = 4; m < 7; ++m) loadA4(m);
      ev_wait(smem, 14);
#pragma unroll
      for (int m = 0; m < 4; ++m) rdB(bp[m], smem + slotA(w, m));
      WAIT_V(0);
#pragma unroll
      for (int m = 4; m < 7; ++m) rdB(bp[m], smem + slotA(w, m));
      STAMP(8);
      if (w == 0) {
        wait_flags(ws, 2, 14, lane);
        ev_signal_all(smem, 13, lane);
      }
    }
    ev_wait(smem, 13);
    STAMP(9);
    const int k0 = grp ? 4 : 11;
#pragma unroll
    for (int m = 0; m < 3; ++m) ldA(m, k0 + m);
    const int tt = rt64 * 4 + (w8 >> 1);
    u32x4 x1v;
    float g1o;
    SB();
#pragma unroll
    for (int m = 0; m < 7; ++m) {
      mma(a[m % 3], bp[m]);
      SB();
      if (m + 3 < 7) ldA(m % 3, k0 + m + 3);
      if (m == 4) {
        x1v = ld16_sc1(rs, OFF_X1 + ((tt * 14 + 2 * c7 + (w8 & 1)) * 64 + lane) * 16);
        g1o = ((const float*)(ws + OFF_G))[(tt * 16 + r16) * 2 + 1];
      }
      SB();
    }
    STAMP(10);
    BAR();
    f32x4* ex = (f32x4*)smem;
#pragma unroll
    for (int pos = 1; pos < 8; ++pos)
#pragma unroll
      for (int h = 0; h < 2; ++h) ex[(((w8 ^ pos) * 7 + pos - 1) * 2 + h) * 64 + lane] = acc[pos >> 1][2 * (pos & 1) + h];
    BAR();
    STAMP(13);
    unsigned pk[2][2];
#pragma unroll
    for (int h = 0; h < 2; ++h) {
      f32x4 r = acc[0][h];
#pragma unroll
      for (int sidx = 0; sidx < 7; ++sidx) r += ex[((w8 * 7 + sidx) * 2 + h) * 64 + lane];
      pk[h][0] = pack2(__builtin_fmaf(g1o, bf_lo(x1v[2 * h]), r[0]), __builtin_fmaf(g1o, bf_hi(x1v[2 * h]), r[1]));
      pk[h][1] = pack2(__builtin_fmaf(g1o, bf_lo(x1v[2 * h + 1]), r[2]), __builtin_fmaf(g1o, bf_hi(x1v[2 * h + 1]), r[3]));
    }
    auto s0 = __builtin_amdgcn_permlane16_swap(pk[0][0], pk[1][0], false, false);
    auto s1 = __builtin_amdgcn_permlane16_swap(pk[0][1], pk[1][1], false, false);
    u32x4 v = {s0[0], s1[0], s0[1], s1[1]};
    const int gl = (g >> 1) | ((g & 1) << 1);
    *(u32x4*)(out + (size_t)(tt * 16 + r16) * D_ + c7 * 64 + 32 * (w8 & 1) + 8 * gl) = v;
  }
  STAMP_FLUSH(ws, 1);
}

extern "C" void kernel_launch(void* const* d_in, const int* in_sizes, int n_in, void* d_out, int out_size, void* d_ws,
                              size_t ws_size, hipStream_t stream) {
  const bf16* x = (const bf16*)d_in[0];
  const bf16* wqkv = (const bf16*)d_in[1];
  const bf16* wo = (const bf16*)d_in[2];
  const bf16* w1 = (const bf16*)d_in[4];
  const bf16* w2 = (const bf16*)d_in[5];
  const bf16* w3 = (const bf16*)d_in[6];
  const bf16* w4 = (const bf16*)d_in[7];
  const bf16* gw = (const bf16*)d_in[8];
  char* ws = (char*)d_ws;
  k1_qkv_h<<<256, 384, 0, stream>>>(x, wqkv, w1, w2, w3, w4, wo, gw, ws);
  k2_mega<<<256, 512, 0, stream>>>(x, w2, w3, w4, wo, ws, (bf16*)d_out);
}
```

```cpp
#include <hip/hip_runtime.h>
#include <hip/hip_bf16.h>

typedef __bf16 bf16;
typedef __bf16 bf16x2 __attribute__((ext_vector_type(2)));
typedef __bf16 bf16x4 __attribute__((ext_vector_type(4)));
typedef __bf16 bf16x8 __attribute__((ext_vector_type(8)));
typedef float f32x2 __attribute__((ext_vector_type(2)));
typedef float f32x4 __attribute__((ext_vector_type(4)));
typedef unsigned u32x2 __attribute__((ext_vector_type(2)));
typedef unsigned u32x4 __attribute__((ext_vector_type(4)));
#define LDS_AS __attribute__((address_space(3)))
#define DI __device__ __forceinline__

constexpr int S_ = 2048, D_ = 448, F_ = 1792;
constexpr size_t OFF_QKV = 0;
constexpr size_t OFF_HS = OFF_QKV + 5505024;
constexpr size_t OFF_X1 = OFF_HS + 1835008;
constexpr size_t OFF_AC = OFF_X1 + 1835008;
constexpr size_t OFF_ST = OFF_AC + 9437184;
constexpr size_t OFF_G  = OFF_ST + 262144;
constexpr size_t OFF_CS = OFF_G + 16384;
constexpr size_t OFF_FL = OFF_CS + 8192;
constexpr size_t OFF_BT = OFF_FL + 3 * 256 * 128;
constexpr size_t WS_USED = OFF_BT + 7 * 72 * 4096;

#define STAMP_DECL
#define STAMP(i)
#define STAMP_FLUSH(ws, kid)
#define STAMP_ARG
#define STAMP_PASS

#define WAIT_V(n) asm volatile("s_waitcnt vmcnt(" #n ")" ::: "memory")
#define BAR() do { asm volatile("s_waitcnt lgkmcnt(0)" ::: "memory"); __builtin_amdgcn_s_barrier(); asm volatile("" ::: "memory"); } while (0)
#define MFMA16(a, b, c) __builtin_amdgcn_mfma_f32_16x16x32_bf16((a), (b), (c), 0, 0, 0)

template <int IMM = 0>
DI void glds16(const void* sbase, unsigned voff, unsigned lds) {
  asm volatile("s_mov_b32 m0, %2\n\ts_nop 0\n\tglobal_load_lds_dwordx4 %0, %1 offset:%3" ::"v"(voff), "s"(sbase), "s"(lds), "i"(IMM) : "memory");
}
template <int LOFF>
DI void glds16c(const void* sbase, unsigned voff, unsigned ldsbase) {
  asm volatile("s_add_u32 m0, %2, %3\n\ts_nop 0\n\tglobal_load_lds_dwordx4 %0, %1" ::"v"(voff), "s"(sbase), "s"(ldsbase), "i"(LOFF) : "memory", "scc");
}
DI void touch8k(unsigned& sink, const void* sbase, unsigned chunk, int lane) {
  asm volatile("global_load_dword %0, %1, %2" : "+v"(sink) : "v"(chunk * 8192u + lane * 128u), "s"(sbase) : "memory");
}
DI void touch_keep(unsigned& sink) { asm volatile("" : "+v"(sink)::"memory"); }
DI unsigned lds_addr(const char* p) { return (unsigned)(size_t)(const LDS_AS char*)p; }
DI bf16x8 ld_frag(const char* lds) { return *(const bf16x8*)lds; }
DI bf16x8 ld_tr2(const char* p0, const char* p1) {
  bf16x4 a = __builtin_amdgcn_ds_read_tr16_b64_v4bf16((LDS_AS bf16x4*)p0);
  bf16x4 b = __builtin_amdgcn_ds_read_tr16_b64_v4bf16((LDS_AS bf16x4*)p1);
  return __builtin_shufflevector(a, b, 0, 1, 2, 3, 4, 5, 6, 7);
}
DI unsigned pack2(float a, float b) {
  f32x2 t = {a, b};
  return __builtin_bit_cast(unsigned, __builtin_convertvector(t, bf16x2));
}
DI float bf_lo(unsigned u) { return __builtin_bit_cast(float, u << 16); }
DI float bf_hi(unsigned u) { return __builtin_bit_cast(float, u & 0xffff0000u); }
DI float xsum4(float x) {
  auto a = __builtin_amdgcn_permlane16_swap(__float_as_uint(x), __float_as_uint(x), false, false);
  x = __uint_as_float(a[0]) + __uint_as_float(a[1]);
  auto b = __builtin_amdgcn_permlane32_swap(__float_as_uint(x), __float_as_uint(x), false, false);
  return __uint_as_float(b[0]) + __uint_as_float(b[1]);
}
DI float xmax4(float x) {
  auto a = __builtin_amdgcn_permlane16_swap(__float_as_uint(x), __float_as_uint(x), false, false);
  x = fmaxf(__uint_as_float(a[0]), __uint_as_float(a[1]));
  auto b = __builtin_amdgcn_permlane32_swap(__float_as_uint(x), __float_as_uint(x), false, false);
  return fmaxf(__uint_as_float(b[0]), __uint_as_float(b[1]));
}

DI float gelu_ln(float z, float k1, float k3, float inv) {
  float t = z * z;
  float w = __builtin_fmaf(t, k3, k1);
  float e = __builtin_amdgcn_exp2f(z * w);
  float d = __builtin_fmaf(e, inv, inv);
  return z * __builtin_amdgcn_rcpf(d);
}
constexpr float GC1 = -2.0f * 0.7978845608028654f * 1.4426950408889634f;
constexpr float GC3 = GC1 * 0.044715f;

DI int swz(int row) { return ((row >> 1) & 1) | ((((row >> 2) ^ (row >> 3)) & 1) << 1); }
DI int src_chunk(int row, int lane) { return ((((lane & 7) >> 1) ^ swz(row)) << 1) | (lane & 1); }

template <int N> DI void wait_v() { asm volatile("s_waitcnt vmcnt(%0)" ::"i"(N) : "memory"); }
#define SB() __builtin_amdgcn_sched_barrier(0)
#define SGB(mask, n) __builtin_amdgcn_sched_group_barrier(mask, n, 0)
DI f32x2 gelu2(f32x2 z, float k1, float k3, float inv) {
  f32x2 t = z * z;
  f32x2 w = __builtin_elementwise_fma(t, f32x2{k3, k3}, f32x2{k1, k1});
  f32x2 u = z * w;
  f32x2 e = {__builtin_amdgcn_exp2f(u[0]), __builtin_amdgcn_exp2f(u[1])};
  f32x2 d = __builtin_elementwise_fma(e, f32x2{inv, inv}, f32x2{inv, inv});
  f32x2 r = {__builtin_amdgcn_rcpf(d[0]), __builtin_amdgcn_rcpf(d[1])};
  return z * r;
}
DI unsigned pack2v(f32x2 t) { return __builtin_bit_cast(unsigned, __builtin_convertvector(t, bf16x2)); }

#define CODE_ADDR(p, sym) do { int off_; asm volatile("s_getpc_b64 %0\n\ts_mov_b32 %1, " #sym "@rel32@lo+4" : "=s"(p), "=s"(off_)); p += off_; } while (0)
constexpr unsigned K2_CODE_BYTES = 26000;

#define NTOUCH 5
constexpr int K1_A = 114688;
template <bool STATS>
DI void k1_compute(char* smem, const char* xrows, int w, int lane, f32x4 (&acc)[2][8], f32x4& cs, f32x4 (&sum)[2],
                   f32x4 (&sq)[2] STAMP_ARG) {
  const int g = lane >> 4, q = (lane >> 2) & 3, p = lane & 3, r16 = lane & 15;
  const char* sa = smem + K1_A + w * 12288;
  const unsigned la = lds_addr(smem) + K1_A + w * 12288;
  const unsigned xoff = ((32 * w + (lane >> 3)) * D_ + (((lane & 7) ^ (lane >> 3)) << 3)) * 2;
  auto issueA = [&](int st, int slot) {
#pragma unroll
    for (int i = 0; i < 4; ++i) glds16(xrows + st * 128, xoff + i * 8 * D_ * 2, la + slot * 4096 + i * 1024);
  };
  issueA(0, 0); issueA(1, 1); issueA(2, 2);
  const int a0 = r16 * 128 + ((g ^ (r16 & 7)) << 4);
  const int b0 = (8 * g + q) * 128 + (swz(8 * g + q) << 5) + p * 8;
  const int b1 = (8 * g + 4 + q) * 128 + (swz(8 * g + 4 + q) << 5) + p * 8;
  bf16x8 ones;
#pragma unroll
  for (int i = 0; i < 8; ++i) ones[i] = (bf16)1.0f;
  bf16x8 A[2][2], B[2][8], Bw[2];
  auto rdA = [&](const char* abase, const char* bbase, int u) {
    A[u][0] = ld_frag(abase + (a0 ^ (u << 6)));
    A[u][1] = ld_frag(abase + (a0 ^ (u << 6)) + 2048);
    if (STATS) Bw[u] = ld_tr2(bbase + 57344 + (b0 ^ (w << 5)), bbase + 57344 + (b1 ^ (w << 5)));
  };
  auto rdB = [&](const char* bbase, int u, int j) {
    const int o = (j >> 2) * 57344;
    B[u][j] = ld_tr2(bbase + o + (b0 ^ ((j & 3) << 5)), bbase + o + (b1 ^ ((j & 3) << 5)));
  };
  auto kstep = [&](int u, const char* nabase, const char* nbbase, bool more) {
#pragma unroll
    for (int j = 0; j < 8; ++j) {
      acc[0][j] = MFMA16(B[u][j], A[u][0], acc[0][j]);
      acc[1][j] = MFMA16(B[u][j], A[u][1], acc[1][j]);
      if (more) { if (j == 0) rdA(nabase, nbbase, u ^ 1); rdB(nbbase, u ^ 1, j); }
      if (STATS) {
        if (j == 1) sum[0] = MFMA16(ones, A[u][0], sum[0]);
        if (j == 2) sum[1] = MFMA16(ones, A[u][1], sum[1]);
        if (j == 4) sq[0] = MFMA16(A[u][0], A[u][0], sq[0]);
        if (j == 5) sq[1] = MFMA16(A[u][1], A[u][1], sq[1]);
        if (j == 7) cs = MFMA16(Bw[u], ones, cs);
      }
      SB();
    }
  };
  STAMP(1);
  WAIT_V(8);
  BAR(); STAMP(2);
  rdA(sa, smem, 0);
#pragma unroll
  for (int j = 0; j < 8; ++j) rdB(smem, 0, j);
  SB();
#pragma unroll
  for (int s = 0; s < 6; ++s) {
    kstep(0, sa + (s % 3) * 4096, smem + s * 8192 + 4096, true);
    if (s < 5) WAIT_V(4); else WAIT_V(0);
    if (s < 4) asm volatile("s_waitcnt lgkmcnt(14)" ::: "memory");
    if (s < 2 || s == 3) __builtin_amdgcn_s_barrier();
    asm volatile("" ::: "memory");
    if (s < 4) issueA(s + 3, s % 3);
    kstep(1, sa + ((s + 1) % 3) * 4096, smem + s * 8192 + 8192, true);
  }
  kstep(0, sa, smem + 6 * 8192 + 4096, true);
  kstep(1, sa, smem, false);
  STAMP(9);
}

extern "C" __global__ __launch_bounds__(384, 2) void k1_qkv_h(const bf16* __restrict__ x, const bf16* __restrict__ wqkv,
                                                   const bf16* __restrict__ w1, const bf16* __restrict__ w2,
                                                   const bf16* __restrict__ w3, const bf16* __restrict__ w4,
                                                   const bf16* __restrict__ wo, const bf16* __restrict__ gw,
                                                   char* __restrict__ ws) {
  __shared__ __attribute__((aligned(16))) char smem[163840];
  const int tid = threadIdx.x, lane = tid & 63;
  const int w = __builtin_amdgcn_readfirstlane(tid >> 6);
  const int xcd = blockIdx.x & 7, idx = blockIdx.x >> 3;
  STAMP_DECL STAMP(0);
  if (idx >= 28) {
    const int hb = xcd * 4 + (idx - 28);
    float* red = (float*)smem;
    if (tid < 192) ((u32x4*)(ws + OFF_FL))[hb * 192 + tid] = u32x4{0u, 0u, 0u, 0u};
    if (w < 4) {
      const int r8 = lane >> 3;
      const unsigned cE = src_chunk(r8, lane) * 16, cO = src_chunk(r8 + 8, lane) * 16;
      const unsigned lt = lds_addr(smem) + 4096 + w * 16384;
#pragma unroll
      for (int m = 0; m < 4; ++m) {
        int u = hb * 16 + w + 4 * m;
        u = u < 504 ? u : 503;
        const int c7 = u / 72, k = u - c7 * 72;
        const char* src = k < 16 ? (const char*)(wo + (size_t)k * 28 * D_ + c7 * 64) : (const char*)(w4 + (size_t)(k - 16) * 32 * D_ + c7 * 64);
#pragma unroll
        for (int i = 0; i < 4; ++i) {
          int d = 8 * i + r8;
          if (k < 16) { d -= 4 * (k & 1); d = d < 0 ? 0 : (d > 27 ? 27 : d); }
          glds16(src, d * (D_ * 2) + ((i & 1) ? cO : cE), lt + m * 4096 + i * 1024);
        }
      }
      if (hb < 28) {
        float a[8] = {};
#pragma unroll
        for (int i = 0; i < 14; ++i) {
          bf16x8 v = *(const bf16x8*)(w3 + (size_t)(w * 112 + i * 8 + (lane >> 3)) * F_ + hb * 64 + (lane & 7) * 8);
#pragma unroll
          for (int e = 0; e < 8; ++e) a[e] += (float)v[e];
        }
#pragma unroll
        for (int e = 0; e < 8; ++e) {
          a[e] += __shfl_xor(a[e], 8);
          a[e] = xsum4(a[e]);
        }
        if (lane < 8) {
#pragma unroll
          for (int e = 0; e < 8; ++e) red[w * 64 + lane * 8 + e] = a[e];
        }
      }
      WAIT_V(0);
      const int g = lane >> 4, q = (lane >> 2) & 3;
      const int btr = (4 * g + q) * 128 + (swz(4 * g + q) << 5) + (lane & 3) * 8;
#pragma unroll
      for (int m = 0; m < 4; ++m) {
        const int u = hb * 16 + w + 4 * m;
        if (u < 504) {
#pragma unroll
          for (int j = 0; j < 4; ++j) {
            const char* bp = smem + 4096 + w * 16384 + m * 4096 + (btr ^ (j << 5));
            *(bf16x8*)(ws + OFF_BT + (size_t)u * 4096 + j * 1024 + lane * 16) = ld_tr2(bp, bp + 2048);
          }
        }
      }
    } else {
      bf16x8 gv = {};
      if (lane < 56) gv = *(const bf16x8*)(gw + lane * 8);
      float* G = (float*)(ws + OFF_G);
      {
        bf16x8 xv[32];
        const int row0 = hb * 64 + (w - 4) * 32;
#pragma unroll
        for (int r = 0; r < 32; ++r) {
          xv[r] = *(const bf16x8*)(x + (row0 + r) * D_ + (lane < 56 ? lane : 0) * 8);
        }
        float mine = 0.f;
#pragma unroll
        for (int r = 0; r < 32; ++r) {
          float s = 0.f;
#pragma unroll
          for (int i = 0; i < 4; ++i) {
            bf16x2 a = {xv[r][2 * i], xv[r][2 * i + 1]}, b = {gv[2 * i], gv[2 * i + 1]};
            s = __builtin_amdgcn_fdot2_f32_bf16(a, b, s, false);
          }
#pragma unroll
          for (int o = 8; o; o >>= 1) s += __shfl_xor(s, o);
          s = xsum4(s);
          mine = lane == r ? s : mine;
        }
        if (lane < 32) {
          f32x2 gg = {1.f / (1.f + __expf(-mine)), 1.f / (1.f + __expf(mine))};
          *(f32x2*)(G + (row0 + lane) * 2) = gg;
        }
      }
      {
        unsigned sink = 0;
        const char* code;
        CODE_ADDR(code, k2_mega);
        const unsigned off = min((unsigned)((((idx & 1) * 2 + w - 4) * 64 + lane) * 128), K2_CODE_BYTES);
        asm volatile("global_load_dword %0, %1, %2" : "+v"(sink) : "v"(off), "s"(code) : "memory");
        WAIT_V(0); touch_keep(sink);
      }
    }
    BAR();
    if (hb < 28 && tid < 64) ((float*)(ws + OFF_CS))[hb * 64 + tid] = (red[tid] + red[64 + tid]) + (red[128 + tid] + red[192 + tid]);
    STAMP_FLUSH(ws, 0);
    return;
  }
  const int panel = 4 * (xcd >> 1) + (idx & 3), ct = 7 * (xcd & 1) + (idx >> 2);
  const bool mixed = ct < 7;
  const int u0 = mixed ? ct : 2 * ct - 7;
  if (w >= 4) {
    const int r = lane >> 3;
    const bool second = w == 5;
    const unsigned l0 = lds_addr(smem) + (second ? 57344 : 0);
    const char* base = (second && mixed) ? (const char*)(w1 + ct * 64) : (const char*)(wqkv + (u0 + (second ? 1 : 0)) * 64);
    const int ld = (second && mixed) ? 896 : 2688;
    const unsigned cE = src_chunk(r, lane) * 16, cO = src_chunk(r + 8, lane) * 16;
    auto issue = [&](int st) {
#pragma unroll
      for (int i = 0; i < 8; ++i) glds16(base + (size_t)st * 64 * ld, (8 * i + r) * ld + ((i & 1) ? cO : cE), l0 + st * 8192 + i * 1024);
    };
    unsigned sink = 0;
#pragma unroll 1
    for (int st = 0; st < 7; ++st) {
      if (st == 2) {
        const char* code;
        CODE_ADDR(code, k1_qkv_h);
        const int c = xcd * 28 + idx;
        const char* pb = second ? code : c < 105 ? (const char*)(wqkv + 128 * 1344) : (const char*)(w1 + 128 * D_);
        touch8k(sink, pb, second ? (idx % 3) : c < 105 ? c : c < 140 ? c - 105 : 0, lane);
        touch8k(sink, (const char*)(x + (size_t)panel * 128 * D_), (idx >> 2) * 2 + (w - 4), lane);
        WAIT_V(10); BAR();
      }
      if (st == 5) { WAIT_V(26); BAR(); }
      issue(st);
    }
    WAIT_V(24); BAR();
    WAIT_V(0); BAR();
    __builtin_amdgcn_s_sleep(4);
#pragma unroll
    for (int m = 0; m < NTOUCH; ++m) {
      const int c = idx * 2 + (w - 4) + 56 * m;
      touch8k(sink, c < 49 ? (const char*)w2 : (const char*)w3, c < 49 ? c : c < 245 ? c - 49 : 0, lane);
    }
    if (mixed) BAR();
    WAIT_V(0);
    touch_keep(sink);
    return;
  }
  const int g = lane >> 4, r16 = lane & 15;
  f32x4 acc[2][8] = {};
  f32x4 cs = {};
  f32x4 sum[2] = {}, sq[2] = {};
  float* ex = (float*)smem;
  const char* xrows = (const char*)(x + (size_t)panel * 128 * D_);
  if (mixed) {
    k1_compute<true>(smem, xrows, w, lane, acc, cs, sum, sq STAMP_PASS);
    if (r16 == 0) *(f32x4*)(ex + w * 16 + 4 * g) = cs;
  } else {
    k1_compute<false>(smem, xrows, w, lane, acc, cs, sum, sq STAMP_PASS);
  }
  {
    const int gl = (g >> 1) | ((g & 1) << 1);
#pragma unroll
    for (int jj = 0; jj < 4; ++jj) {
      if (jj < 2 || !mixed) {
#pragma unroll
        for (int rf = 0; rf < 2; ++rf) {
          const int tt = panel * 8 + 2 * w + rf;
          unsigned a0 = pack2(acc[rf][2 * jj][0], acc[rf][2 * jj][1]), a1 = pack2(acc[rf][2 * jj][2], acc[rf][2 * jj][3]);
          unsigned b0 = pack2(acc[rf][2 * jj + 1][0], acc[rf][2 * jj + 1][1]), b1 = pack2(acc[rf][2 * jj + 1][2], acc[rf][2 * jj + 1][3]);
          auto s0 = __builtin_amdgcn_permlane16_swap(a0, b0, false, false);
          auto s1 = __builtin_amdgcn_permlane16_swap(a1, b1, false, false);
          u32x4 v = {s0[0], s1[0], s0[1], s1[1]};
          *(u32x4*)(ws + OFF_QKV + ((size_t)((tt * 168 + u0 * 8 + 4 * jj + gl) * 16 + r16) << 4)) = v;
        }
      }
    }
  }
  if (mixed) {
    BAR();
    const int nb = ct * 64;
    f32x4 c4[4];
#pragma unroll
    for (int j = 0; j < 4; ++j) c4[j] = *(const f32x4*)(ex + j * 16 + 4 * g);
    unsigned hv[2][4][2];
#pragma unroll
    for (int rf = 0; rf < 2; ++rf) {
      float mean = sum[rf][0] * (1.f / D_);
      float dg = (r16 & 2) ? ((r16 & 1) ? sq[rf][3] : sq[rf][2]) : ((r16 & 1) ? sq[rf][1] : sq[rf][0]);
      dg = xsum4(g == (r16 >> 2) ? dg : 0.f);
      float var = fmaxf(dg * (1.f / D_) - mean * mean, 0.f);
      float a = __builtin_amdgcn_rsqf(var + 1e-5f);
      float k1 = GC1 * a, k3 = GC3 * a * a * a, inv = __builtin_amdgcn_rcpf(a);
      const f32x2 nm = {-mean, -mean};
#pragma unroll
      for (int j = 0; j < 4; ++j) {
        f32x2 z0 = __builtin_elementwise_fma(nm, f32x2{c4[j][0], c4[j][1]}, f32x2{acc[rf][4 + j][0], acc[rf][4 + j][1]});
        f32x2 z1 = __builtin_elementwise_fma(nm, f32x2{c4[j][2], c4[j][3]}, f32x2{acc[rf][4 + j][2], acc[rf][4 + j][3]});
        hv[rf][j][0] = pack2v(gelu2(z0, k1, k3, inv));
        hv[rf][j][1] = pack2v(gelu2(z1, k1, k3, inv));
      }
    }
#pragma unroll
    for (int rf = 0; rf < 2; ++rf)
#pragma unroll
      for (int j = 0; j < 4; ++j) asm volatile("" : "+v"(hv[rf][j][0]), "+v"(hv[rf][j][1]));
#pragma unroll
    for (int jj = 0; jj < 2; ++jj) {
      const int ks = (nb >> 5) + jj;
      int sh[2];
#pragma unroll
      for (int h = 0; h < 2; ++h) {
        const int grp = (nb + 32 * jj + 16 * h) / 112;
        sh[h] = (grp & 2 ? 2 : 1) * (grp & 1 ? -1 : 1);
      }
#pragma unroll
      for (int rf = 0; rf < 2; ++rf) {
        const int tok = panel * 128 + 32 * w + 16 * rf + r16;
        int t2[2];
        u32x2 v[2];
#pragma unroll
        for (int h = 0; h < 2; ++h) {
          t2[h] = tok + sh[h];
          const bool ok = (unsigned)t2[h] < (unsigned)S_;
          t2[h] &= S_ - 1;
          v[h][0] = ok ? hv[rf][2 * jj + h][0] : 0u;
          v[h][1] = ok ? hv[rf][2 * jj + h][1] : 0u;
        }
        char* d0 = ws + OFF_HS + ((size_t)(((t2[0] >> 4) * 14 + ks) * 64 + g * 16 + (t2[0] & 15)) << 4);
        if (ks != 3 && ks != 10) {
          u32x4 vv = {v[0][0], v[0][1], v[1][0], v[1][1]};
          *(u32x4*)d0 = vv;
        } else {
          char* d1 = ws + OFF_HS + ((size_t)(((t2[1] >> 4) * 14 + ks) * 64 + g * 16 + (t2[1] & 15)) << 4) + 8;
          *(u32x2*)d0 = v[0];
          *(u32x2*)d1 = v[1];
        }
      }
    }
  }
  STAMP(10);
  STAMP_FLUSH(ws, 0);
}

typedef __attribute__((address_space(1))) unsigned gu32;
typedef __amdgpu_buffer_rsrc_t rsrc_t;
DI u32x4 ld16_sc1(rsrc_t r, unsigned off) { return __builtin_amdgcn_raw_buffer_load_b128(r, off, 0, 16); }
DI u32x4 ld16(rsrc_t r, unsigned off) { return __builtin_amdgcn_raw_buffer_load_b128(r, off, 0, 0); }
DI void st16_sc1(rsrc_t r, unsigned off, u32x4 v) { __builtin_amdgcn_raw_buffer_store_b128(v, r, off, 0, 16); }
DI void st8_sc1(rsrc_t r, unsigned off, u32x2 v) { __builtin_amdgcn_raw_buffer_store_b64(v, r, off, 0, 16); }
DI bf16x8 as_frag(u32x4 v) { return __builtin_bit_cast(bf16x8, v); }
DI void post_flags(char* ws, int kind, int consumer, int word, int lane) {
  if (lane < 14)
    __hip_atomic_store((gu32*)(ws + OFF_FL) + (kind * 256 + consumer) * 32 + word, 1u, __ATOMIC_RELAXED, __HIP_MEMORY_SCOPE_AGENT);
}
DI void wait_flags(char* ws, int kind, int nwords, int lane) {
  gu32* f = (gu32*)(ws + OFF_FL) + (kind * 256 + blockIdx.x) * 32 + (lane < nwords ? lane : 0);
#pragma unroll 1
  for (int spin = 0; spin < (1 << 18); ++spin) {
    unsigned v = __hip_atomic_load(f, __ATOMIC_RELAXED, __HIP_MEMORY_SCOPE_AGENT);
    if (__all(v == 1u)) break;
    __builtin_amdgcn_s_sleep(2);
  }
  asm volatile("" ::: "memory");
}
constexpr int L_H1 = 57344, L_X = 102400, L_EV = 110592, L_C = 114688;
DI void ev_signal(char* smem, int ev, int wq, int lane) {
  asm volatile("s_waitcnt lgkmcnt(0)" ::: "memory");
  if (lane == 0) *(volatile LDS_AS unsigned*)(LDS_AS char*)(smem + L_EV + ev * 16 + wq * 4) = 1u;
  asm volatile("" ::: "memory");
}
DI void ev_wait(char* smem, int ev) {
  asm volatile("" ::: "memory");
#pragma unroll 1
  for (int spin = 0; spin < (1 << 22); ++spin) {
    u32x4 v = *(volatile LDS_AS u32x4*)(LDS_AS char*)(smem + L_EV + ev * 16);
    if (__builtin_amdgcn_readfirstlane(v[0] & v[1] & v[2] & v[3])) break;
    __builtin_amdgcn_s_sleep(1);
  }
  asm volatile("" ::: "memory");
}
DI void ev_signal_all(char* smem, int ev, int lane) {
  asm volatile("" ::: "memory");
  if (lane < 4) *(volatile LDS_AS unsigned*)(LDS_AS char*)(smem + L_EV + ev * 16 + lane * 4) = 1u;
  asm volatile("" ::: "memory");
}
extern "C" __global__ __launch_bounds__(512, 2) void k2_mega(const bf16* __restrict__ x, const bf16* __restrict__ w2,
                                                  const bf16* __restrict__ w3, const bf16* __restrict__ w4,
                                                  const bf16* __restrict__ wo, char* __restrict__ ws,
                                                  bf16* __restrict__ out) {
  __shared__ __attribute__((aligned(16))) char smem[163840];
  const int tid = threadIdx.x, lane = tid & 63;
  const int w8 = __builtin_amdgcn_readfirstlane(tid >> 6);
  const int grp = w8 >> 2, w = w8 & 3;
  const int xcd = blockIdx.x & 7, idx = blockIdx.x >> 3;
  STAMP_DECL STAMP(0);
  const unsigned l0 = lds_addr(smem);
  const bool has_gemm = idx < 28;
  const int rt64 = 4 * xcd + (idx & 3), c7 = idx >> 2;
  const int panel = 2 * xcd + (idx & 1), c14 = idx >> 1;
  const int head = c14;
  const int g = lane >> 4, q = (lane >> 2) & 3, p = lane & 3, r16 = lane & 15, r8 = lane >> 3;
  const rsrc_t rs = __builtin_amdgcn_make_buffer_rsrc(ws, 0, (int)WS_USED, 0x00020000);
  const int btr = (4 * g + q) * 128 + (swz(4 * g + q) << 5) + p * 8;
  const unsigned cE = src_chunk(r8, lane) * 16, cO = src_chunk(r8 + 8, lane) * 16;
  auto loadW = [&](const char* base, int ld, int g0, int n, unsigned dst) {
    const unsigned oE = r8 * ld + cE, oO = (r8 + 8) * ld + cO;
#pragma unroll 1
    for (int m = g0; m < g0 + n; ++m) {
      glds16(base + (size_t)m * 16 * ld, oE, dst + m * 2048);
      glds16(base + (size_t)m * 16 * ld, oO, dst + m * 2048 + 1024);
    }
  };
  const char* w3b = (const char*)(w3 + c14 * 128);
  bf16x8 bp[7][4];
  const unsigned boff = OFF_BT + (c7 * 72 + w) * 4096 + lane * 16;
  auto ldB = [&](bf16x8 (&bf)[4], int k4) {
#pragma unroll
    for (int j = 0; j < 4; ++j) bf[j] = as_frag(ld16(rs, (boff ^ ((w8 & 1) << 11) ^ (j << 10)) + k4 * 16384));
  };
  if (tid < 128) ((unsigned*)(smem + L_EV))[tid] = 0u;
  const int cons3 = ((2 * lane + ((rt64 >> 1) & 1)) << 3) | xcd;
  const int cons4 = ((4 * (lane >> 1) + 2 * (panel & 1) + (lane & 1)) << 3) | xcd;

  if (grp == 0) {
    if (!has_gemm) {
      BAR();
      unsigned sink = 0;
      const int wv = (idx - 28) * 4 + w;
      __builtin_amdgcn_s_sleep(32);
#pragma unroll 1
      for (int m = 0; m < 16; ++m) {
        const int c = wv + 16 * m;
        touch8k(sink, ws + OFF_BT, c < 252 ? c : 0, lane);
        WAIT_V(1);
      }
      WAIT_V(0); touch_keep(sink);
      return;
    }
    __builtin_amdgcn_s_setprio(2);
    loadW((const char*)(w2 + c7 * 64), D_ * 2, 7 * w, 7, l0);
    {
      const char* bx = (const char*)(x + (size_t)(rt64 * 64 + 16 * w) * D_ + c7 * 64);
#pragma unroll
      for (int i = 0; i < 2; ++i) glds16(bx, ((8 * i + r8) * D_ + (((lane & 7) ^ r8) << 3)) * 2, l0 + L_X + w * 2048 + i * 1024);
    }
    u32x4 ha[14];
#pragma unroll
    for (int ks = 0; ks < 14; ++ks) ha[ks] = ld16(rs, OFF_HS + (((rt64 * 4 + w) * 14 + ks) * 64 + lane) * 16);
    BAR();
    WAIT_V(14);
    ev_signal(smem, 0, w, lane);
    ev_wait(smem, 0);
    STAMP(1);
    {
      f32x4 acc[4] = {};
      bf16x8 bq[3][4];
      auto rdB = [&](int ks, int j) {
        const char* bp = smem + ks * 4096 + (btr ^ (j << 5));
        bq[ks % 3][j] = ld_tr2(bp, bp + 2048);
      };
#pragma unroll
      for (int j = 0; j < 4; ++j) { rdB(0, j); rdB(1, j); }
      const int trow = 16 * w + r16;
      u32x2 xr[4];
      SB();
#pragma unroll
      for (int ks = 0; ks < 14; ++ks) {
#pragma unroll
        for (int j = 0; j < 4; ++j) {
          acc[j] = MFMA16(bq[ks % 3][j], as_frag(ha[ks]), acc[j]);
          if (ks + 2 < 14) rdB(ks + 2, j);
          if (ks == 12) xr[j] = *(const u32x2*)(smem + L_X + trow * 128 + (((2 * j + (g >> 1)) ^ (trow & 7)) << 4) + (g & 1) * 8);
          SB();
        }
      }
      STAMP(11);
      float sum = 0.f, sq = 0.f;
      u32x4 x1k[2];
#pragma unroll
      for (int j = 0; j < 4; ++j) {
        unsigned lo = pack2(acc[j][0] + bf_lo(xr[j][0]), acc[j][1] + bf_hi(xr[j][0]));
        unsigned hi = pack2(acc[j][2] + bf_lo(xr[j][1]), acc[j][3] + bf_hi(xr[j][1]));
        x1k[j >> 1][2 * (j & 1)] = lo; x1k[j >> 1][2 * (j & 1) + 1] = hi;
        float e0 = bf_lo(lo), e1 = bf_hi(lo), e2 = bf_lo(hi), e3 = bf_hi(hi);
        sum += (e0 + e1) + (e2 + e3);
        sq += (e0 * e0 + e1 * e1) + (e2 * e2 + e3 * e3);
      }
#pragma unroll
      for (int jj = 0; jj < 2; ++jj) st16_sc1(rs, OFF_X1 + (((rt64 * 4 + w) * 14 + 2 * c7 + jj) * 64 + lane) * 16, x1k[jj]);
      u32x2 t = {__float_as_uint(xsum4(sum)), __float_as_uint(xsum4(sq))};
      if (g == 0) st8_sc1(rs, OFF_ST + (rt64 * 4 + w) * 1024 + (c7 >> 1) * 256 + r16 * 16 + (c7 & 1) * 8, t);
    }
    WAIT_V(0);
    ev_signal(smem, 8, w, lane);
    ev_wait(smem, 8);
    if (w == 0) post_flags(ws, 0, cons3, (rt64 & 1) * 7 + c7, lane);
    STAMP(2);
    loadW(w3b, F_ * 2, 7 * w, 7, l0);
    loadW(w3b + 128, F_ * 2, 22 + w, 1, l0 + L_H1);
    WAIT_V(0);
    STAMP(3);
    ev_signal(smem, 2, w, lane);
    if (w == 0) {
      wait_flags(ws, 0, 14, lane);
      ev_signal_all(smem, 9, lane);
    } else {
      ev_wait(smem, 9);
    }
    STAMP(4);
    {
      u32x4 a[2][14];
      const unsigned aoff = OFF_X1 + ((panel * 8 + 2 * w) * 14 * 64 + lane) * 16;
      auto ldA = [&](int ks) {
        a[0][ks] = ld16_sc1(rs, aoff + ks * 1024);
        a[1][ks] = ld16_sc1(rs, aoff + 14336 + ks * 1024);
      };
#pragma unroll
      for (int ks = 0; ks < 6; ++ks) ldA(ks);
      ev_wait(smem, 2);
      ev_wait(smem, 6);
      STAMP(5);
      u32x4 stv[2];
      float g1[2];
      f32x4 c4[8];
      f32x4 acc[2][8] = {};
      bf16x8 bq[8];
      auto rdB = [&](int n) {
        const int ks = n >> 3, j = n & 7;
        const int base = j < 4 ? ks * 4096 : (ks < 13 ? L_H1 + ks * 4096 : L_C);
        const char* bp = smem + base + (btr ^ ((j & 3) << 5));
        return ld_tr2(bp, bp + 2048);
      };
#pragma unroll
      for (int n = 0; n < 6; ++n) bq[n] = rdB(n);
      SB();
#pragma unroll
      for (int ks = 0; ks < 14; ++ks) {
        if (ks == 12) ev_wait(smem, 7);
        if (ks == 6 && lane == 0) *(volatile LDS_AS unsigned*)(LDS_AS char*)(smem + L_EV + 15 * 16 + w * 4) = 1u;
#pragma unroll
        for (int j = 0; j < 8; ++j) {
          const int n = ks * 8 + j;
          acc[0][j] = MFMA16(bq[j], as_frag(a[0][ks]), acc[0][j]);
          acc[1][j] = MFMA16(bq[j], as_frag(a[1][ks]), acc[1][j]);
          if (n + 6 < 112) bq[(j + 6) & 7] = rdB(n + 6);
          if ((j == 1 || j == 5) && ks < 4) ldA(2 * ks + 6 + (j >> 2));
          if (ks == 4 && j < 2) {
            stv[j] = ld16_sc1(rs, OFF_ST + (panel * 8 + 2 * w + j) * 1024 + lane * 16);
            g1[j] = ((const float*)(ws + OFF_G))[(panel * 128 + 32 * w + 16 * j + r16) * 2 + 1];
          }
          if (ks == 5) c4[j] = *(const f32x4*)(ws + OFF_CS + (size_t)(c14 * 128 + 16 * j + 4 * g) * 4);
          SB();
        }
      }
      ev_signal(smem, 3, w, lane);
      STAMP(6);
#pragma unroll
      for (int rf = 0; rf < 2; ++rf) {
        float s1 = __uint_as_float(stv[rf][0]), s2 = __uint_as_float(stv[rf][1]);
        if (g < 3) { s1 += __uint_as_float(stv[rf][2]); s2 += __uint_as_float(stv[rf][3]); }
        s1 = xsum4(s1); s2 = xsum4(s2);
        float mean = s1 * (1.f / D_);
        float var = fmaxf(s2 * (1.f / D_) - mean * mean, 0.f);
        float a_ = __builtin_amdgcn_rsqf(var + 1e-5f);
        float k1 = GC1 * a_, k3 = GC3 * a_ * a_ * a_, inv = __builtin_amdgcn_rcpf(fmaxf(a_ * g1[rf], 1e-30f));
        const f32x2 nm = {-mean, -mean};
#pragma unroll
        for (int jj = 0; jj < 4; ++jj) {
          u32x4 v;
#pragma unroll
          for (int h = 0; h < 2; ++h) {
            const int j = 2 * jj + h;
            f32x2 z0 = __builtin_elementwise_fma(nm, f32x2{c4[j][0], c4[j][1]}, f32x2{acc[rf][j][0], acc[rf][j][1]});
            f32x2 z1 = __builtin_elementwise_fma(nm, f32x2{c4[j][2], c4[j][3]}, f32x2{acc[rf][j][2], acc[rf][j][3]});
            v[2 * h] = pack2v(gelu2(z0, k1, k3, inv));
            v[2 * h + 1] = pack2v(gelu2(z1, k1, k3, inv));
          }
          st16_sc1(rs, OFF_AC + (((panel * 8 + 2 * w + rf) * 72 + 16 + c14 * 4 + jj) * 64 + lane) * 16, v);
        }
      }
    }
    STAMP(7);
    WAIT_V(0);
    STAMP(12);
    ev_signal(smem, 10, w, lane);
    if (w == 0) {
      ev_wait(smem, 10);
      post_flags(ws, 2, cons4, c14, lane);
    }
    __builtin_amdgcn_s_setprio(0);
    ev_wait(smem, 14);
#pragma unroll
    for (int m = 0; m < 7; ++m) {
      const int n = 7 * w + m;
      const char* sl = smem + (n < 27 ? n * 4096 : L_C) + lane * 16;
#pragma unroll
      for (int j = 0; j < 4; ++j) bp[m][j] = ld_frag(sl + ((j << 10) ^ ((w8 & 1) << 11)));
    }
    ev_signal(smem, 16, w, lane);
    STAMP(8);
  } else {
    const int gb = (7 * head) >> 1;
    const int qw = panel * 128 + 32 * w;
    BAR();
    __builtin_amdgcn_s_setprio(3);
    {
      const unsigned lv = (lane & 48) | ((lane & 15) ^ ((lane & 16) >> 1));
      const char* src = ws + OFF_QKV + (size_t)(56 + gb) * 256;
#pragma unroll
      for (int m = 0; m < 6; ++m) {
        const int lt = 6 * w + m;
        int kt = panel * 8 - 16 + lt;
        kt = kt < 0 ? 0 : kt;
        glds16(src + (size_t)kt * 43008, lane * 16, l0 + L_C + lt * 1024);
        glds16(src + (size_t)kt * 43008 + 56 * 256, lv * 16, l0 + L_C + 24576 + lt * 1024);
      }
    }
    u32x4 qv[2];
#pragma unroll
    for (int h = 0; h < 2; ++h)
      asm volatile("global_load_dwordx4 %0, %1, %2" : "=v"(qv[h]) : "v"((((panel * 8 + 2 * w + h) * 168 + gb) * 16 + lane) * 16), "s"(ws + OFF_QKV) : "memory");
    if (has_gemm) {
      const unsigned o0 = (88 * w + r8) * (F_ * 2) + ((w & 1) ? cO : cE), o1 = (88 * w + r8) * (F_ * 2) + ((w & 1) ? cE : cO);
      const unsigned ld = l0 + L_H1 + w * 11264;
#pragma unroll
      for (int m = 0; m < 11; ++m) glds16(w3b + 128, ((m & 1) ? o1 : o0) + m * 8 * F_ * 2, ld + m * 1024);
      WAIT_V(11);
    } else {
      WAIT_V(0);
    }
    asm volatile("" : "+v"(qv[0]), "+v"(qv[1])::"memory");
    ev_signal(smem, 4, w, lane);
    ev_wait(smem, 4);
    STAMP(1);
    const char* sK = smem + L_C;
    const char* sV = smem + L_C + 24576;
    const bool odd = head & 1;
    const bool z01 = odd && g == 0, z23 = !odd && g == 3;
    bf16x8 qf[2];
#pragma unroll
    for (int h = 0; h < 2; ++h) {
      qv[h][0] = z01 ? 0u : qv[h][0]; qv[h][1] = z01 ? 0u : qv[h][1];
      qv[h][2] = z23 ? 0u : qv[h][2]; qv[h][3] = z23 ? 0u : qv[h][3];
      qf[h] = as_frag(qv[h]);
    }
    const float NEG = -__builtin_inff();
    constexpr float SC = 0.1889822365046136f * 1.4426950408889634f;
    f32x4 s[2][17];
    float mx[2] = {NEG, NEG};
    auto max4 = [&](float m, const f32x4& v) {
      return __builtin_fmaxf(__builtin_fmaxf(__builtin_fmaxf(__builtin_fmaxf(m, v[0]), v[1]), v[2]), v[3]);
    };
    auto finish = [&](int h, int t) {
      if (t == 0 || t == 16) {
#pragma unroll
        for (int i = 0; i < 4; ++i) {
          const int b = 4 * g + i;
          s[h][t][i] = ((b > r16) == (t == 0)) ? s[h][t][i] : NEG;
        }
      }
      mx[h] = max4(mx[h], s[h][t]);
    };
    {
      bf16x8 kf[6];
#pragma unroll
      for (int t = 0; t < 6; ++t) kf[t] = ld_frag(sK + (2 * w + t) * 1024 + lane * 16);
      SB();
#pragma unroll
      for (int t = 0; t < 20; ++t) {
        if (t < 18) {
          f32x4 z = {};
          if (t < 17) s[0][t] = MFMA16(kf[t % 6], qf[0], z);
          if (t > 0) s[1][t - 1] = MFMA16(kf[t % 6], qf[1], z);
          if (t + 6 < 18) kf[t % 6] = ld_frag(sK + (2 * w + t + 6) * 1024 + lane * 16);
        }
        if (t >= 2) {
          if (t - 2 < 17) finish(0, t - 2);
          if (t - 2 > 0) finish(1, t - 3);
        }
        SB();
      }
    }
    if (qw < 256) {
#pragma unroll
      for (int h = 0; h < 2; ++h) {
        const int tstart = 16 - (qw >> 4) - h;
        mx[h] = NEG;
#pragma unroll
        for (int t = 0; t < 17; ++t) {
          if (t < tstart) s[h][t] = f32x4{NEG, NEG, NEG, NEG};
          mx[h] = max4(mx[h], s[h][t]);
        }
      }
    }
    const float mc[2] = {-xmax4(mx[0]) * SC, -xmax4(mx[1]) * SC};
    STAMP(2);
    WAIT_V(0);
    ev_signal(smem, 6, w, lane);
    float gt[2];
#pragma unroll
    for (int h = 0; h < 2; ++h) gt[h] = ((const float*)(ws + OFF_G))[(qw + 16 * h + r16) * 2];
    STAMP(3);
    f32x4 o[2][2] = {}, lsum[2] = {};
    bf16x8 ones;
#pragma unroll
    for (int i = 0; i < 8; ++i) ones[i] = (bf16)1.0f;
    auto pexp = [&](const f32x4& sv, float m, unsigned& lo, unsigned& hi) {
      f32x2 a = __builtin_elementwise_fma(f32x2{sv[0], sv[1]}, f32x2{SC, SC}, f32x2{m, m});
      f32x2 b = __builtin_elementwise_fma(f32x2{sv[2], sv[3]}, f32x2{SC, SC}, f32x2{m, m});
      lo = pack2(__builtin_amdgcn_exp2f(a[0]), __builtin_amdgcn_exp2f(a[1]));
      hi = pack2(__builtin_amdgcn_exp2f(b[0]), __builtin_amdgcn_exp2f(b[1]));
    };
    const int voff0 = (p >> 1) * 256 + (((4 * g + q) ^ ((p >> 1) << 3)) << 4) + (p & 1) * 8;
    bf16x8 vf[2][2], pf[2][2];
#pragma unroll
    for (int kk = 0; kk < 10; ++kk) {
      if (kk < 9) {
        const char* vb = sV + (2 * w + 2 * kk) * 1024 + voff0;
        vf[kk & 1][0] = ld_tr2(vb, vb + 1024);
        vf[kk & 1][1] = ld_tr2(vb + 512, vb + 1536);
#pragma unroll
        for (int h = 0; h < 2; ++h) {
          unsigned p0 = 0, p1 = 0, p2 = 0, p3 = 0;
          const int t0 = 2 * kk - h, t1 = t0 + 1;
          if (t0 >= 0) pexp(s[h][t0 < 0 ? 0 : t0], mc[h], p0, p1);
          if (t1 <= 16) pexp(s[h][t1 > 16 ? 16 : t1], mc[h], p2, p3);
          pf[kk & 1][h] = as_frag(u32x4{p0, p1, p2, p3});
        }
      }
      if (kk > 0) {
        const int c = (kk - 1) & 1;
#pragma unroll
        for (int h = 0; h < 2; ++h) {
          o[h][0] = MFMA16(vf[c][0], pf[c][h], o[h][0]);
          o[h][1] = MFMA16(vf[c][1], pf[c][h], o[h][1]);
          lsum[h] = MFMA16(ones, pf[c][h], lsum[h]);
        }
      }
      SGB(0x100, 4);
#pragma unroll
      for (int m = 0; m < 6; ++m) { SGB(0x8, 1); SGB(0x402, 5); }
      SB();
    }
    __builtin_amdgcn_s_setprio(3);
    ev_signal(smem, 5, w, lane);
    STAMP(4);
#pragma unroll
    for (int h = 0; h < 2; ++h) {
      float sc = gt[h] * __builtin_amdgcn_rcpf(lsum[h][0]);
      u32x4 v;
      v[0] = z01 ? 0u : pack2(o[h][0][0] * sc, o[h][0][1] * sc);
      v[1] = z01 ? 0u : pack2(o[h][0][2] * sc, o[h][0][3] * sc);
      v[2] = z23 ? 0u : pack2(o[h][1][0] * sc, o[h][1][1] * sc);
      v[3] = z23 ? 0u : pack2(o[h][1][2] * sc, o[h][1][3] * sc);
      st16_sc1(rs, OFF_AC + (((panel * 8 + 2 * w + h) * 72 + head) * 64 + lane) * 16, v);
    }
    if (!has_gemm) {
      WAIT_V(0);
      ev_signal(smem, 11, w, lane);
      if (w == 0) {
        ev_wait(smem, 11);
        post_flags(ws, 1, cons4, head, lane);
      }
      return;
    }
    ev_wait(smem, 5);
    glds16(w3b + 128 + (size_t)(416 + 8 * w) * F_ * 2, r8 * (F_ * 2) + ((w & 1) ? cO : cE), l0 + L_C + w * 1024);
  }
  {
    unsigned aoff[4];
#pragma unroll
    for (int rt = 0; rt < 4; ++rt) aoff[rt] = OFF_AC + (((rt64 * 4 + (rt ^ (w8 >> 1))) * 72 + w) * 64 + lane) * 16;
    f32x4 acc[4][4] = {};
    u32x4 a[3][4];
    auto ldA = [&](int buf, int k4) {
#pragma unroll
      for (int rt = 0; rt < 4; ++rt) a[buf][rt] = ld16_sc1(rs, aoff[rt] + k4 * 4096);
    };
    auto mma = [&](const u32x4 (&af)[4], const bf16x8 (&bf)[4]) {
#pragma unroll
      for (int rt = 0; rt < 4; ++rt)
#pragma unroll
        for (int j = 0; j < 4; ++j) acc[rt][j] = MFMA16(bf[j], as_frag(af[rt]), acc[rt][j]);
    };
    if (grp == 1) {
      bf16x8 bo[4];
      WAIT_V(0);
      ev_signal(smem, 7, w, lane);
      STAMP(5);
      if (w == 0) {
        ev_wait(smem, 7);
        post_flags(ws, 1, cons4, head, lane);
      }
      ev_wait(smem, 15);
      ldB(bp[6], 0); ldB(bo, 1);
#pragma unroll
      for (int m = 0; m < 6; ++m) ldB(bp[m], 4 + m);
      if (w == 0) {
        wait_flags(ws, 1, 16, lane);
        ev_signal_all(smem, 12, lane);
      } else {
        ev_wait(smem, 12);
      }
      STAMP(6);
      ldA(0, 0); ldA(1, 1);
      __builtin_amdgcn_s_setprio(0);
      SB();
      mma(a[0], bp[6]);
      SB();
      ldB(bp[6], 2); ldA(0, 2);
      SB();
      mma(a[1], bo);
      SB();
      ldB(bo, 3); ldA(1, 3);
      SB();
      mma(a[0], bp[6]);
      SB();
      ldB(bp[6], 10);
      SB();
      mma(a[1], bo);
      SB();
      STAMP(7);
#pragma unroll
      for (int j = 0; j < 4; ++j) asm volatile("" : "+v"(bp[6][j]));
      ev_wait(smem, 3);
#pragma unroll
      for (int m = 0; m < 7; ++m) {
        const int n = 7 * w + m;
        const unsigned dst = l0 + (n < 27 ? n * 4096 : L_C);
        const char* src = ws + OFF_BT + (size_t)(c7 * 72 + 44 + w + 4 * m) * 4096;
        glds16<0>(src, lane * 16, dst); glds16<1024>(src, lane * 16, dst);
        glds16<2048>(src, lane * 16, dst); glds16<3072>(src, lane * 16, dst);
        WAIT_V(4);
      }
      WAIT_V(0);
      ev_signal(smem, 14, w, lane);
      STAMP(8);
      if (w == 0) {
        wait_flags(ws, 2, 14, lane);
        ev_signal_all(smem, 13, lane);
      }
    }
    ev_wait(smem, 13);
    STAMP(9);
    const int k0 = grp ? 4 : 11;
#pragma unroll
    for (int m = 0; m < 3; ++m) ldA(m, k0 + m);
    const int tt = rt64 * 4 + (w8 >> 1);
    u32x4 x1v;
    float g1o;
    SB();
#pragma unroll
    for (int m = 0; m < 7; ++m) {
      mma(a[m % 3], bp[m]);
      SB();
      if (m + 3 < 7) ldA(m % 3, k0 + m + 3);
      if (m == 4) {
        x1v = ld16_sc1(rs, OFF_X1 + ((tt * 14 + 2 * c7 + (w8 & 1)) * 64 + lane) * 16);
        g1o = ((const float*)(ws + OFF_G))[(tt * 16 + r16) * 2 + 1];
      }
      SB();
    }
    STAMP(10);
    ev_wait(smem, 16);
    auto exa = [&](int dst) { return (f32x4*)(smem + dst * 14336 + (dst == 7 ? L_C - 7 * 14336 : 0)) + lane; };
#pragma unroll
    for (int pos = 1; pos < 8; ++pos)
#pragma unroll
      for (int h = 0; h < 2; ++h) exa(w8 ^ pos)[((pos - 1) * 2 + h) * 64] = acc[pos >> 1][2 * (pos & 1) + h];
    BAR();
    STAMP(13);
    unsigned pk[2][2];
#pragma unroll
    for (int h = 0; h < 2; ++h) {
      f32x4 r = acc[0][h];
#pragma unroll
      for (int sidx = 0; sidx < 7; ++sidx) r += exa(w8)[(sidx * 2 + h) * 64];
      pk[h][0] = pack2(__builtin_fmaf(g1o, bf_lo(x1v[2 * h]), r[0]), __builtin_fmaf(g1o, bf_hi(x1v[2 * h]), r[1]));
      pk[h][1] = pack2(__builtin_fmaf(g1o, bf_lo(x1v[2 * h + 1]), r[2]), __builtin_fmaf(g1o, bf_hi(x1v[2 * h + 1]), r[3]));
    }
    auto s0 = __builtin_amdgcn_permlane16_swap(pk[0][0], pk[1][0], false, false);
    auto s1 = __builtin_amdgcn_permlane16_swap(pk[0][1], pk[1][1], false, false);
    u32x4 v = {s0[0], s1[0], s0[1], s1[1]};
    const int gl = (g >> 1) | ((g & 1) << 1);
    *(u32x4*)(out + (size_t)(tt * 16 + r16) * D_ + c7 * 64 + 32 * (w8 & 1) + 8 * gl) = v;
  }
  STAMP_FLUSH(ws, 1);
}

extern "C" void kernel_launch(void* const* d_in, const int* in_sizes, int n_in, void* d_out, int out_size, void* d_ws,
                              size_t ws_size, hipStream_t stream) {
  const bf16* x = (const bf16*)d_in[0];
  const bf16* wqkv = (const bf16*)d_in[1];
  const bf16* wo = (const bf16*)d_in[2];
  const bf16* w1 = (const bf16*)d_in[4];
  const bf16* w2 = (const bf16*)d_in[5];
  const bf16* w3 = (const bf16*)d_in[6];
  const bf16* w4 = (const bf16*)d_in[7];
  const bf16* gw = (const bf16*)d_in[8];
  char* ws = (char*)d_ws;
  k1_qkv_h<<<256, 384, 0, stream>>>(x, wqkv, w1, w2, w3, w4, wo, gw, ws);
  k2_mega<<<256, 512, 0, stream>>>(x, w2, w3, w4, wo, ws, (bf16*)d_out);
}
```

```cpp
#include <hip/hip_runtime.h>
#include <hip/hip_bf16.h>

typedef __bf16 bf16;
typedef __bf16 bf16x2 __attribute__((ext_vector_type(2)));
typedef __bf16 bf16x4 __attribute__((ext_vector_type(4)));
typedef __bf16 bf16x8 __attribute__((ext_vector_type(8)));
typedef float f32x2 __attribute__((ext_vector_type(2)));
typedef float f32x4 __attribute__((ext_vector_type(4)));
typedef unsigned u32x2 __attribute__((ext_vector_type(2)));
typedef unsigned u32x4 __attribute__((ext_vector_type(4)));
#define LDS_AS __attribute__((address_space(3)))
#define DI __device__ __forceinline__

constexpr int S_ = 2048, D_ = 448, F_ = 1792;
constexpr size_t OFF_QKV = 0;
constexpr size_t OFF_HS = OFF_QKV + 5505024;
constexpr size_t OFF_X1 = OFF_HS + 1835008;
constexpr size_t OFF_AC = OFF_X1 + 1835008;
constexpr size_t OFF_ST = OFF_AC + 9437184;
constexpr size_t OFF_G  = OFF_ST + 262144;
constexpr size_t OFF_CS = OFF_G + 16384;
constexpr size_t OFF_FL = OFF_CS + 8192;
constexpr size_t OFF_BT = OFF_FL + 3 * 256 * 128;
constexpr size_t WS_USED = OFF_BT + 7 * 72 * 4096;

#define STAMP_DECL
#define STAMP(i)
#define STAMP_FLUSH(ws, kid)
#define STAMP_ARG
#define STAMP_PASS

#define WAIT_V(n) asm volatile("s_waitcnt vmcnt(" #n ")" ::: "memory")
#define BAR() do { asm volatile("s_waitcnt lgkmcnt(0)" ::: "memory"); __builtin_amdgcn_s_barrier(); asm volatile("" ::: "memory"); } while (0)
#define MFMA16(a, b, c) __builtin_amdgcn_mfma_f32_16x16x32_bf16((a), (b), (c), 0, 0, 0)

template <int IMM = 0>
DI void glds16(const void* sbase, unsigned voff, unsigned lds) {
  asm volatile("s_mov_b32 m0, %2\n\ts_nop 0\n\tglobal_load_lds_dwordx4 %0, %1 offset:%3" ::"v"(voff), "s"(sbase), "s"(lds), "i"(IMM) : "memory");
}
template <int LOFF>
DI void glds16c(const void* sbase, unsigned voff, unsigned ldsbase) {
  asm volatile("s_add_u32 m0, %2, %3\n\ts_nop 0\n\tglobal_load_lds_dwordx4 %0, %1" ::"v"(voff), "s"(sbase), "s"(ldsbase), "i"(LOFF) : "memory", "scc");
}
DI void touch8k(unsigned& sink, const void* sbase, unsigned chunk, int lane) {
  asm volatile("global_load_dword %0, %1, %2" : "+v"(sink) : "v"(chunk * 8192u + lane * 128u), "s"(sbase) : "memory");
}
DI void touch_keep(unsigned& sink) { asm volatile("" : "+v"(sink)::"memory"); }
DI unsigned lds_addr(const char* p) { return (unsigned)(size_t)(const LDS_AS char*)p; }
DI bf16x8 ld_frag(const char* lds) { return *(const bf16x8*)lds; }
DI bf16x8 ld_tr2(const char* p0, const char* p1) {
  bf16x4 a = __builtin_amdgcn_ds_read_tr16_b64_v4bf16((LDS_AS bf16x4*)p0);
  bf16x4 b = __builtin_amdgcn_ds_read_tr16_b64_v4bf16((LDS_AS bf16x4*)p1);
  return __builtin_shufflevector(a, b, 0, 1, 2, 3, 4, 5, 6, 7);
}
DI unsigned pack2(float a, float b) {
  f32x2 t = {a, b};
  return __builtin_bit_cast(unsigned, __builtin_convertvector(t, bf16x2));
}
DI float bf_lo(unsigned u) { return __builtin_bit_cast(float, u << 16); }
DI float bf_hi(unsigned u) { return __builtin_bit_cast(float, u & 0xffff0000u); }
DI float xsum4(float x) {
  auto a = __builtin_amdgcn_permlane16_swap(__float_as_uint(x), __float_as_uint(x), false, false);
  x = __uint_as_float(a[0]) + __uint_as_float(a[1]);
  auto b = __builtin_amdgcn_permlane32_swap(__float_as_uint(x), __float_as_uint(x), false, false);
  return __uint_as_float(b[0]) + __uint_as_float(b[1]);
}
DI float xmax4(float x) {
  auto a = __builtin_amdgcn_permlane16_swap(__float_as_uint(x), __float_as_uint(x), false, false);
  x = fmaxf(__uint_as_float(a[0]), __uint_as_float(a[1]));
  auto b = __builtin_amdgcn_permlane32_swap(__float_as_uint(x), __float_as_uint(x), false, false);
  return fmaxf(__uint_as_float(b[0]), __uint_as_float(b[1]));
}

DI float gelu_ln(float z, float k1, float k3, float inv) {
  float t = z * z;
  float w = __builtin_fmaf(t, k3, k1);
  float e = __builtin_amdgcn_exp2f(z * w);
  float d = __builtin_fmaf(e, inv, inv);
  return z * __builtin_amdgcn_rcpf(d);
}
constexpr float GC1 = -2.0f * 0.7978845608028654f * 1.4426950408889634f;
constexpr float GC3 = GC1 * 0.044715f;

DI int swz(int row) { return ((row >> 1) & 1) | ((((row >> 2) ^ (row >> 3)) & 1) << 1); }
DI int src_chunk(int row, int lane) { return ((((lane & 7) >> 1) ^ swz(row)) << 1) | (lane & 1); }

template <int N> DI void wait_v() { asm volatile("s_waitcnt vmcnt(%0)" ::"i"(N) : "memory"); }
#define SB() __builtin_amdgcn_sched_barrier(0)
#define SGB(mask, n) __builtin_amdgcn_sched_group_barrier(mask, n, 0)
DI f32x2 gelu2(f32x2 z, float k1, float k3, float inv) {
  f32x2 t = z * z;
  f32x2 w = __builtin_elementwise_fma(t, f32x2{k3, k3}, f32x2{k1, k1});
  f32x2 u = z * w;
  f32x2 e = {__builtin_amdgcn_exp2f(u[0]), __builtin_amdgcn_exp2f(u[1])};
  f32x2 d = __builtin_elementwise_fma(e, f32x2{inv, inv}, f32x2{inv, inv});
  f32x2 r = {__builtin_amdgcn_rcpf(d[0]), __builtin_amdgcn_rcpf(d[1])};
  return z * r;
}
DI unsigned pack2v(f32x2 t) { return __builtin_bit_cast(unsigned, __builtin_convertvector(t, bf16x2)); }

#define CODE_ADDR(p, sym) do { int off_; asm volatile("s_getpc_b64 %0\n\ts_mov_b32 %1, " #sym "@rel32@lo+4" : "=s"(p), "=s"(off_)); p += off_; } while (0)
constexpr unsigned K2_CODE_BYTES = 26000;

#define NTOUCH 5
constexpr int K1_A = 114688;
template <bool STATS>
DI void k1_compute(char* smem, const char* xrows, int w, int lane, f32x4 (&acc)[2][8], f32x4& cs, f32x4 (&sum)[2],
                   f32x4 (&sq)[2] STAMP_ARG) {
  const int g = lane >> 4, q = (lane >> 2) & 3, p = lane & 3, r16 = lane & 15;
  const char* sa = smem + K1_A + w * 12288;
  const unsigned la = lds_addr(smem) + K1_A + w * 12288;
  const unsigned xoff = ((32 * w + (lane >> 3)) * D_ + (((lane & 7) ^ (lane >> 3)) << 3)) * 2;
  auto issueA = [&](int st, int slot) {
#pragma unroll
    for (int i = 0; i < 4; ++i) glds16(xrows + st * 128, xoff + i * 8 * D_ * 2, la + slot * 4096 + i * 1024);
  };
  issueA(0, 0); issueA(1, 1); issueA(2, 2);
  const int a0 = r16 * 128 + ((g ^ (r16 & 7)) << 4);
  const int b0 = (8 * g + q) * 128 + (swz(8 * g + q) << 5) + p * 8;
  const int b1 = (8 * g + 4 + q) * 128 + (swz(8 * g + 4 + q) << 5) + p * 8;
  bf16x8 ones;
#pragma unroll
  for (int i = 0; i < 8; ++i) ones[i] = (bf16)1.0f;
  bf16x8 A[2][2], B[2][8], Bw[2];
  auto rdA = [&](const char* abase, const char* bbase, int u) {
    A[u][0] = ld_frag(abase + (a0 ^ (u << 6)));
    A[u][1] = ld_frag(abase + (a0 ^ (u << 6)) + 2048);
    if (STATS) Bw[u] = ld_tr2(bbase + 57344 + (b0 ^ (w << 5)), bbase + 57344 + (b1 ^ (w << 5)));
  };
  auto rdB = [&](const char* bbase, int u, int j) {
    const int o = (j >> 2) * 57344;
    B[u][j] = ld_tr2(bbase + o + (b0 ^ ((j & 3) << 5)), bbase + o + (b1 ^ ((j & 3) << 5)));
  };
  auto kstep = [&](int u, const char* nabase, const char* nbbase, bool more) {
#pragma unroll
    for (int j = 0; j < 8; ++j) {
      acc[0][j] = MFMA16(B[u][j], A[u][0], acc[0][j]);
      acc[1][j] = MFMA16(B[u][j], A[u][1], acc[1][j]);
      if (more) { if (j == 0) rdA(nabase, nbbase, u ^ 1); rdB(nbbase, u ^ 1, j); }
      if (STATS) {
        if (j == 1) sum[0] = MFMA16(ones, A[u][0], sum[0]);
        if (j == 2) sum[1] = MFMA16(ones, A[u][1], sum[1]);
        if (j == 4) sq[0] = MFMA16(A[u][0], A[u][0], sq[0]);
        if (j == 5) sq[1] = MFMA16(A[u][1], A[u][1], sq[1]);
        if (j == 7) cs = MFMA16(Bw[u], ones, cs);
      }
      SB();
    }
  };
  STAMP(1);
  WAIT_V(8);
  BAR(); STAMP(2);
  rdA(sa, smem, 0);
#pragma unroll
  for (int j = 0; j < 8; ++j) rdB(smem, 0, j);
  SB();
#pragma unroll
  for (int s = 0; s < 6; ++s) {
    kstep(0, sa + (s % 3) * 4096, smem + s * 8192 + 4096, true);
    if (s < 5) WAIT_V(4); else WAIT_V(0);
    if (s < 4) asm volatile("s_waitcnt lgkmcnt(14)" ::: "memory");
    if (s < 2 || s == 3) __builtin_amdgcn_s_barrier();
    asm volatile("" ::: "memory");
    if (s < 4) issueA(s + 3, s % 3);
    kstep(1, sa + ((s + 1) % 3) * 4096, smem + s * 8192 + 8192, true);
  }
  kstep(0, sa, smem + 6 * 8192 + 4096, true);
  kstep(1, sa, smem, false);
  STAMP(9);
}

extern "C" __global__ __launch_bounds__(384, 2) void k1_qkv_h(const bf16* __restrict__ x, const bf16* __restrict__ wqkv,
                                                   const bf16* __restrict__ w1, const bf16* __restrict__ w2,
                                                   const bf16* __restrict__ w3, const bf16* __restrict__ w4,
                                                   const bf16* __restrict__ wo, const bf16* __restrict__ gw,
                                                   char* __restrict__ ws) {
  __shared__ __attribute__((aligned(16))) char smem[163840];
  const int tid = threadIdx.x, lane = tid & 63;
  const int w = __builtin_amdgcn_readfirstlane(tid >> 6);
  const int xcd = blockIdx.x & 7, idx = blockIdx.x >> 3;
  STAMP_DECL STAMP(0);
  if (idx >= 28) {
    const int hb = xcd * 4 + (idx - 28);
    float* red = (float*)smem;
    if (tid < 192) ((u32x4*)(ws + OFF_FL))[hb * 192 + tid] = u32x4{0u, 0u, 0u, 0u};
    if (w < 4) {
      const int r8 = lane >> 3;
      const unsigned cE = src_chunk(r8, lane) * 16, cO = src_chunk(r8 + 8, lane) * 16;
      const unsigned lt = lds_addr(smem) + 4096 + w * 16384;
#pragma unroll
      for (int m = 0; m < 4; ++m) {
        int u = hb * 16 + w + 4 * m;
        u = u < 504 ? u : 503;
        const int c7 = u / 72, k = u - c7 * 72;
        const char* src = k < 16 ? (const char*)(wo + (size_t)k * 28 * D_ + c7 * 64) : (const char*)(w4 + (size_t)(k - 16) * 32 * D_ + c7 * 64);
#pragma unroll
        for (int i = 0; i < 4; ++i) {
          int d = 8 * i + r8;
          if (k < 16) { d -= 4 * (k & 1); d = d < 0 ? 0 : (d > 27 ? 27 : d); }
          glds16(src, d * (D_ * 2) + ((i & 1) ? cO : cE), lt + m * 4096 + i * 1024);
        }
      }
      if (hb < 28) {
        float a[8] = {};
#pragma unroll
        for (int i = 0; i < 14; ++i) {
          bf16x8 v = *(const bf16x8*)(w3 + (size_t)(w * 112 + i * 8 + (lane >> 3)) * F_ + hb * 64 + (lane & 7) * 8);
#pragma unroll
          for (int e = 0; e < 8; ++e) a[e] += (float)v[e];
        }
#pragma unroll
        for (int e = 0; e < 8; ++e) {
          a[e] += __shfl_xor(a[e], 8);
          a[e] = xsum4(a[e]);
        }
        if (lane < 8) {
#pragma unroll
          for (int e = 0; e < 8; ++e) red[w * 64 + lane * 8 + e] = a[e];
        }
      }
      WAIT_V(0);
      const int g = lane >> 4, q = (lane >> 2) & 3;
      const int btr = (4 * g + q) * 128 + (swz(4 * g + q) << 5) + (lane & 3) * 8;
#pragma unroll
      for (int m = 0; m < 4; ++m) {
        const int u = hb * 16 + w + 4 * m;
        if (u < 504) {
#pragma unroll
          for (int j = 0; j < 4; ++j) {
            const char* bp = smem + 4096 + w * 16384 + m * 4096 + (btr ^ (j << 5));
            *(bf16x8*)(ws + OFF_BT + (size_t)u * 4096 + j * 1024 + lane * 16) = ld_tr2(bp, bp + 2048);
          }
        }
      }
    } else {
      bf16x8 gv = {};
      if (lane < 56) gv = *(const bf16x8*)(gw + lane * 8);
      float* G = (float*)(ws + OFF_G);
      {
        bf16x8 xv[32];
        const int row0 = hb * 64 + (w - 4) * 32;
#pragma unroll
        for (int r = 0; r < 32; ++r) {
          xv[r] = *(const bf16x8*)(x + (row0 + r) * D_ + (lane < 56 ? lane : 0) * 8);
        }
        float mine = 0.f;
#pragma unroll
        for (int r = 0; r < 32; ++r) {
          float s = 0.f;
#pragma unroll
          for (int i = 0; i < 4; ++i) {
            bf16x2 a = {xv[r][2 * i], xv[r][2 * i + 1]}, b = {gv[2 * i], gv[2 * i + 1]};
            s = __builtin_amdgcn_fdot2_f32_bf16(a, b, s, false);
          }
#pragma unroll
          for (int o = 8; o; o >>= 1) s += __shfl_xor(s, o);
          s = xsum4(s);
          mine = lane == r ? s : mine;
        }
        if (lane < 32) {
          f32x2 gg = {1.f / (1.f + __expf(-mine)), 1.f / (1.f + __expf(mine))};
          *(f32x2*)(G + (row0 + lane) * 2) = gg;
        }
      }
      {
        unsigned sink = 0;
        const char* code;
        CODE_ADDR(code, k2_mega);
        const unsigned off = min((unsigned)((((idx & 1) * 2 + w - 4) * 64 + lane) * 128), K2_CODE_BYTES);
        asm volatile("global_load_dword %0, %1, %2" : "+v"(sink) : "v"(off), "s"(code) : "memory");
        WAIT_V(0); touch_keep(sink);
      }
    }
    BAR();
    if (hb < 28 && tid < 64) ((float*)(ws + OFF_CS))[hb * 64 + tid] = (red[tid] + red[64 + tid]) + (red[128 + tid] + red[192 + tid]);
    STAMP_FLUSH(ws, 0);
    return;
  }
  const int panel = 4 * (xcd >> 1) + (idx & 3), ct = 7 * (xcd & 1) + (idx >> 2);
  const bool mixed = ct < 7;
  const int u0 = mixed ? ct : 2 * ct - 7;
  if (w >= 4) {
    const int r = lane >> 3;
    const bool second = w == 5;
    const unsigned l0 = lds_addr(smem) + (second ? 57344 : 0);
    const char* base = (second && mixed) ? (const char*)(w1 + ct * 64) : (const char*)(wqkv + (u0 + (second ? 1 : 0)) * 64);
    const int ld = (second && mixed) ? 896 : 2688;
    const unsigned cE = src_chunk(r, lane) * 16, cO = src_chunk(r + 8, lane) * 16;
    auto issue = [&](int st) {
#pragma unroll
      for (int i = 0; i < 8; ++i) glds16(base + (size_t)st * 64 * ld, (8 * i + r) * ld + ((i & 1) ? cO : cE), l0 + st * 8192 + i * 1024);
    };
    unsigned sink = 0;
#pragma unroll 1
    for (int st = 0; st < 7; ++st) {
      if (st == 2) {
        const char* code;
        CODE_ADDR(code, k1_qkv_h);
        const int c = xcd * 28 + idx;
        const char* pb = second ? code : c < 105 ? (const char*)(wqkv + 128 * 1344) : (const char*)(w1 + 128 * D_);
        touch8k(sink, pb, second ? (idx % 3) : c < 105 ? c : c < 140 ? c - 105 : 0, lane);
        touch8k(sink, (const char*)(x + (size_t)panel * 128 * D_), (idx >> 2) * 2 + (w - 4), lane);
        WAIT_V(10); BAR();
      }
      if (st == 5) { WAIT_V(26); BAR(); }
      issue(st);
    }
    WAIT_V(24); BAR();
    WAIT_V(0); BAR();
    __builtin_amdgcn_s_sleep(4);
#pragma unroll
    for (int m = 0; m < NTOUCH; ++m) {
      const int c = idx * 2 + (w - 4) + 56 * m;
      touch8k(sink, c < 49 ? (const char*)w2 : (const char*)w3, c < 49 ? c : c < 245 ? c - 49 : 0, lane);
    }
    if (mixed) BAR();
    WAIT_V(0);
    touch_keep(sink);
    return;
  }
  const int g = lane >> 4, r16 = lane & 15;
  f32x4 acc[2][8] = {};
  f32x4 cs = {};
  f32x4 sum[2] = {}, sq[2] = {};
  float* ex = (float*)smem;
  const char* xrows = (const char*)(x + (size_t)panel * 128 * D_);
  if (mixed) {
    k1_compute<true>(smem, xrows, w, lane, acc, cs, sum, sq STAMP_PASS);
    if (r16 == 0) *(f32x4*)(ex + w * 16 + 4 * g) = cs;
  } else {
    k1_compute<false>(smem, xrows, w, lane, acc, cs, sum, sq STAMP_PASS);
  }
  {
    const int gl = (g >> 1) | ((g & 1) << 1);
#pragma unroll
    for (int jj = 0; jj < 4; ++jj) {
      if (jj < 2 || !mixed) {
#pragma unroll
        for (int rf = 0; rf < 2; ++rf) {
          const int tt = panel * 8 + 2 * w + rf;
          unsigned a0 = pack2(acc[rf][2 * jj][0], acc[rf][2 * jj][1]), a1 = pack2(acc[rf][2 * jj][2], acc[rf][2 * jj][3]);
          unsigned b0 = pack2(acc[rf][2 * jj + 1][0], acc[rf][2 * jj + 1][1]), b1 = pack2(acc[rf][2 * jj + 1][2], acc[rf][2 * jj + 1][3]);
          auto s0 = __builtin_amdgcn_permlane16_swap(a0, b0, false, false);
          auto s1 = __builtin_amdgcn_permlane16_swap(a1, b1, false, false);
          u32x4 v = {s0[0], s1[0], s0[1], s1[1]};
          *(u32x4*)(ws + OFF_QKV + ((size_t)((tt * 168 + u0 * 8 + 4 * jj + gl) * 16 + r16) << 4)) = v;
        }
      }
    }
  }
  if (mixed) {
    BAR();
    const int nb = ct * 64;
    f32x4 c4[4];
#pragma unroll
    for (int j = 0; j < 4; ++j) c4[j] = *(const f32x4*)(ex + j * 16 + 4 * g);
    unsigned hv[2][4][2];
#pragma unroll
    for (int rf = 0; rf < 2; ++rf) {
      float mean = sum[rf][0] * (1.f / D_);
      float dg = (r16 & 2) ? ((r16 & 1) ? sq[rf][3] : sq[rf][2]) : ((r16 & 1) ? sq[rf][1] : sq[rf][0]);
      dg = xsum4(g == (r16 >> 2) ? dg : 0.f);
      float var = fmaxf(dg * (1.f / D_) - mean * mean, 0.f);
      float a = __builtin_amdgcn_rsqf(var + 1e-5f);
      float k1 = GC1 * a, k3 = GC3 * a * a * a, inv = __builtin_amdgcn_rcpf(a);
      const f32x2 nm = {-mean, -mean};
#pragma unroll
      for (int j = 0; j < 4; ++j) {
        f32x2 z0 = __builtin_elementwise_fma(nm, f32x2{c4[j][0], c4[j][1]}, f32x2{acc[rf][4 + j][0], acc[rf][4 + j][1]});
        f32x2 z1 = __builtin_elementwise_fma(nm, f32x2{c4[j][2], c4[j][3]}, f32x2{acc[rf][4 + j][2], acc[rf][4 + j][3]});
        hv[rf][j][0] = pack2v(gelu2(z0, k1, k3, inv));
        hv[rf][j][1] = pack2v(gelu2(z1, k1, k3, inv));
      }
    }
#pragma unroll
    for (int rf = 0; rf < 2; ++rf)
#pragma unroll
      for (int j = 0; j < 4; ++j) asm volatile("" : "+v"(hv[rf][j][0]), "+v"(hv[rf][j][1]));
#pragma unroll
    for (int jj = 0; jj < 2; ++jj) {
      const int ks = (nb >> 5) + jj;
      int sh[2];
#pragma unroll
      for (int h = 0; h < 2; ++h) {
        const int grp = (nb + 32 * jj + 16 * h) / 112;
        sh[h] = (grp & 2 ? 2 : 1) * (grp & 1 ? -1 : 1);
      }
#pragma unroll
      for (int rf = 0; rf < 2; ++rf) {
        const int tok = panel * 128 + 32 * w + 16 * rf + r16;
        int t2[2];
        u32x2 v[2];
#pragma unroll
        for (int h = 0; h < 2; ++h) {
          t2[h] = tok + sh[h];
          const bool ok = (unsigned)t2[h] < (unsigned)S_;
          t2[h] &= S_ - 1;
          v[h][0] = ok ? hv[rf][2 * jj + h][0] : 0u;
          v[h][1] = ok ? hv[rf][2 * jj + h][1] : 0u;
        }
        char* d0 = ws + OFF_HS + ((size_t)(((t2[0] >> 4) * 14 + ks) * 64 + g * 16 + (t2[0] & 15)) << 4);
        if (ks != 3 && ks != 10) {
          u32x4 vv = {v[0][0], v[0][1], v[1][0], v[1][1]};
          *(u32x4*)d0 = vv;
        } else {
          char* d1 = ws + OFF_HS + ((size_t)(((t2[1] >> 4) * 14 + ks) * 64 + g * 16 + (t2[1] & 15)) << 4) + 8;
          *(u32x2*)d0 = v[0];
          *(u32x2*)d1 = v[1];
        }
      }
    }
  }
  STAMP(10);
  STAMP_FLUSH(ws, 0);
}

typedef __attribute__((address_space(1))) unsigned gu32;
typedef __amdgpu_buffer_rsrc_t rsrc_t;
DI u32x4 ld16_sc1(rsrc_t r, unsigned off) { return __builtin_amdgcn_raw_buffer_load_b128(r, off, 0, 16); }
DI u32x4 ld16(rsrc_t r, unsigned off) { return __builtin_amdgcn_raw_buffer_load_b128(r, off, 0, 0); }
DI void st16_sc1(rsrc_t r, unsigned off, u32x4 v) { __builtin_amdgcn_raw_buffer_store_b128(v, r, off, 0, 16); }
DI void st8_sc1(rsrc_t r, unsigned off, u32x2 v) { __builtin_amdgcn_raw_buffer_store_b64(v, r, off, 0, 16); }
DI bf16x8 as_frag(u32x4 v) { return __builtin_bit_cast(bf16x8, v); }
DI void post_flags(char* ws, int kind, int consumer, int word, int lane) {
  if (lane < 14)
    __hip_atomic_store((gu32*)(ws + OFF_FL) + (kind * 256 + consumer) * 32 + word, 1u, __ATOMIC_RELAXED, __HIP_MEMORY_SCOPE_AGENT);
}
DI void wait_flags(char* ws, int kind, int nwords, int lane) {
  gu32* f = (gu32*)(ws + OFF_FL) + (kind * 256 + blockIdx.x) * 32 + (lane < nwords ? lane : 0);
#pragma unroll 1
  for (int spin = 0; spin < (1 << 18); ++spin) {
    unsigned v = __hip_atomic_load(f, __ATOMIC_RELAXED, __HIP_MEMORY_SCOPE_AGENT);
    if (__all(v == 1u)) break;
    __builtin_amdgcn_s_sleep(2);
  }
  asm volatile("" ::: "memory");
}
constexpr int L_H1 = 57344, L_X = 102400, L_EV = 110592, L_C = 114688;
DI void ev_signal(char* smem, int ev, int wq, int lane) {
  asm volatile("s_waitcnt lgkmcnt(0)" ::: "memory");
  if (lane == 0) *(volatile LDS_AS unsigned*)(LDS_AS char*)(smem + L_EV + ev * 16 + wq * 4) = 1u;
  asm volatile("" ::: "memory");
}
DI void ev_wait(char* smem, int ev) {
  asm volatile("" ::: "memory");
#pragma unroll 1
  for (int spin = 0; spin < (1 << 22); ++spin) {
    u32x4 v = *(volatile LDS_AS u32x4*)(LDS_AS char*)(smem + L_EV + ev * 16);
    if (__builtin_amdgcn_readfirstlane(v[0] & v[1] & v[2] & v[3])) break;
    __builtin_amdgcn_s_sleep(2);
  }
  asm volatile("" ::: "memory");
}
DI void ev_signal_all(char* smem, int ev, int lane) {
  asm volatile("" ::: "memory");
  if (lane < 4) *(volatile LDS_AS unsigned*)(LDS_AS char*)(smem + L_EV + ev * 16 + lane * 4) = 1u;
  asm volatile("" ::: "memory");
}
extern "C" __global__ __launch_bounds__(512, 2) void k2_mega(const bf16* __restrict__ x, const bf16* __restrict__ w2,
                                                  const bf16* __restrict__ w3, const bf16* __restrict__ w4,
                                                  const bf16* __restrict__ wo, char* __restrict__ ws,
                                                  bf16* __restrict__ out) {
  __shared__ __attribute__((aligned(16))) char smem[163840];
  const int tid = threadIdx.x, lane = tid & 63;
  const int w8 = __builtin_amdgcn_readfirstlane(tid >> 6);
  const int grp = w8 >> 2, w = w8 & 3;
  const int xcd = blockIdx.x & 7, idx = blockIdx.x >> 3;
  STAMP_DECL STAMP(0);
  const unsigned l0 = lds_addr(smem);
  const bool has_gemm = idx < 28;
  const int rt64 = 4 * xcd + (idx & 3), c7 = idx >> 2;
  const int panel = 2 * xcd + (idx & 1), c14 = idx >> 1;
  const int head = c14;
  const int g = lane >> 4, q = (lane >> 2) & 3, p = lane & 3, r16 = lane & 15, r8 = lane >> 3;
  const rsrc_t rs = __builtin_amdgcn_make_buffer_rsrc(ws, 0, (int)WS_USED, 0x00020000);
  const int btr = (4 * g + q) * 128 + (swz(4 * g + q) << 5) + p * 8;
  const unsigned cE = src_chunk(r8, lane) * 16, cO = src_chunk(r8 + 8, lane) * 16;
  auto loadW = [&](const char* base, int ld, int g0, int n, unsigned dst) {
    const unsigned oE = r8 * ld + cE, oO = (r8 + 8) * ld + cO;
#pragma unroll 1
    for (int m = g0; m < g0 + n; ++m) {
      glds16(base + (size_t)m * 16 * ld, oE, dst + m * 2048);
      glds16(base + (size_t)m * 16 * ld, oO, dst + m * 2048 + 1024);
    }
  };
  const char* w3b = (const char*)(w3 + c14 * 128);
  bf16x8 bp[7][4];
  const unsigned boff = OFF_BT + (c7 * 72 + w) * 4096 + lane * 16;
  auto ldB = [&](bf16x8 (&bf)[4], int k4) {
#pragma unroll
    for (int j = 0; j < 4; ++j) bf[j] = as_frag(ld16(rs, (boff ^ ((w8 & 1) << 11) ^ (j << 10)) + k4 * 16384));
  };
  if (tid < 128) ((unsigned*)(smem + L_EV))[tid] = 0u;
  const int cons3 = ((2 * lane + ((rt64 >> 1) & 1)) << 3) | xcd;
  const int cons4 = ((4 * (lane >> 1) + 2 * (panel & 1) + (lane & 1)) << 3) | xcd;

  if (grp == 0) {
    if (!has_gemm) {
      BAR();
      unsigned sink = 0;
      const int wv = (idx - 28) * 4 + w;
      __builtin_amdgcn_s_sleep(32);
#pragma unroll 1
      for (int m = 0; m < 16; ++m) {
        const int c = wv + 16 * m;
        touch8k(sink, ws + OFF_BT, c < 252 ? c : 0, lane);
        WAIT_V(1);
      }
      WAIT_V(0); touch_keep(sink);
      return;
    }
    __builtin_amdgcn_s_setprio(2);
    loadW((const char*)(w2 + c7 * 64), D_ * 2, 7 * w, 7, l0);
    {
      const char* bx = (const char*)(x + (size_t)(rt64 * 64 + 16 * w) * D_ + c7 * 64);
#pragma unroll
      for (int i = 0; i < 2; ++i) glds16(bx, ((8 * i + r8) * D_ + (((lane & 7) ^ r8) << 3)) * 2, l0 + L_X + w * 2048 + i * 1024);
    }
    u32x4 ha[14];
#pragma unroll
    for (int ks = 0; ks < 14; ++ks) ha[ks] = ld16(rs, OFF_HS + (((rt64 * 4 + w) * 14 + ks) * 64 + lane) * 16);
    BAR();
    WAIT_V(14);
    ev_signal(smem, 0, w, lane);
    ev_wait(smem, 0);
    STAMP(1);
    {
      f32x4 acc[4] = {};
      bf16x8 bq[3][4];
      auto rdB = [&](int ks, int j) {
        const char* bp = smem + ks * 4096 + (btr ^ (j << 5));
        bq[ks % 3][j] = ld_tr2(bp, bp + 2048);
      };
#pragma unroll
      for (int j = 0; j < 4; ++j) { rdB(0, j); rdB(1, j); }
      const int trow = 16 * w + r16;
      u32x2 xr[4];
      SB();
#pragma unroll
      for (int ks = 0; ks < 14; ++ks) {
#pragma unroll
        for (int j = 0; j < 4; ++j) {
          acc[j] = MFMA16(bq[ks % 3][j], as_frag(ha[ks]), acc[j]);
          if (ks + 2 < 14) rdB(ks + 2, j);
          if (ks == 12) xr[j] = *(const u32x2*)(smem + L_X + trow * 128 + (((2 * j + (g >> 1)) ^ (trow & 7)) << 4) + (g & 1) * 8);
          SB();
        }
      }
      STAMP(11);
      float sum = 0.f, sq = 0.f;
      u32x4 x1k[2];
#pragma unroll
      for (int j = 0; j < 4; ++j) {
        unsigned lo = pack2(acc[j][0] + bf_lo(xr[j][0]), acc[j][1] + bf_hi(xr[j][0]));
        unsigned hi = pack2(acc[j][2] + bf_lo(xr[j][1]), acc[j][3] + bf_hi(xr[j][1]));
        x1k[j >> 1][2 * (j & 1)] = lo; x1k[j >> 1][2 * (j & 1) + 1] = hi;
        float e0 = bf_lo(lo), e1 = bf_hi(lo), e2 = bf_lo(hi), e3 = bf_hi(hi);
        sum += (e0 + e1) + (e2 + e3);
        sq += (e0 * e0 + e1 * e1) + (e2 * e2 + e3 * e3);
      }
#pragma unroll
      for (int jj = 0; jj < 2; ++jj) st16_sc1(rs, OFF_X1 + (((rt64 * 4 + w) * 14 + 2 * c7 + jj) * 64 + lane) * 16, x1k[jj]);
      u32x2 t = {__float_as_uint(xsum4(sum)), __float_as_uint(xsum4(sq))};
      if (g == 0) st8_sc1(rs, OFF_ST + (rt64 * 4 + w) * 1024 + (c7 >> 1) * 256 + r16 * 16 + (c7 & 1) * 8, t);
    }
    WAIT_V(0);
    ev_signal(smem, 8, w, lane);
    ev_wait(smem, 8);
    if (w == 0) post_flags(ws, 0, cons3, (rt64 & 1) * 7 + c7, lane);
    STAMP(2);
    loadW(w3b, F_ * 2, 7 * w, 7, l0);
    loadW(w3b + 128, F_ * 2, 22 + w, 1, l0 + L_H1);
    WAIT_V(0);
    STAMP(3);
    ev_signal(smem, 2, w, lane);
    if (w == 0) {
      wait_flags(ws, 0, 14, lane);
      ev_signal_all(smem, 9, lane);
    } else {
      ev_wait(smem, 9);
    }
    STAMP(4);
    {
      u32x4 a[2][14];
      const unsigned aoff = OFF_X1 + ((panel * 8 + 2 * w) * 14 * 64 + lane) * 16;
      auto ldA = [&](int ks) {
        a[0][ks] = ld16_sc1(rs, aoff + ks * 1024);
        a[1][ks] = ld16_sc1(rs, aoff + 14336 + ks * 1024);
      };
#pragma unroll
      for (int ks = 0; ks < 6; ++ks) ldA(ks);
      ev_wait(smem, 2);
      ev_wait(smem, 6);
      STAMP(5);
      u32x4 stv[2];
      float g1[2];
      f32x4 c4[8];
      f32x4 acc[2][8] = {};
      bf16x8 bq[8];
      auto rdB = [&](int n) {
        const int ks = n >> 3, j = n & 7;
        const int base = j < 4 ? ks * 4096 : (ks < 13 ? L_H1 + ks * 4096 : L_C);
        const char* bp = smem + base + (btr ^ ((j & 3) << 5));
        return ld_tr2(bp, bp + 2048);
      };
#pragma unroll
      for (int n = 0; n < 6; ++n) bq[n] = rdB(n);
      SB();
#pragma unroll
      for (int ks = 0; ks < 14; ++ks) {
        if (ks == 12) ev_wait(smem, 7);
        if (ks == 6 && lane == 0) *(volatile LDS_AS unsigned*)(LDS_AS char*)(smem + L_EV + 15 * 16 + w * 4) = 1u;
#pragma unroll
        for (int j = 0; j < 8; ++j) {
          const int n = ks * 8 + j;
          acc[0][j] = MFMA16(bq[j], as_frag(a[0][ks]), acc[0][j]);
          acc[1][j] = MFMA16(bq[j], as_frag(a[1][ks]), acc[1][j]);
          if (n + 6 < 112) bq[(j + 6) & 7] = rdB(n + 6);
          if ((j == 1 || j == 5) && ks < 4) ldA(2 * ks + 6 + (j >> 2));
          if (ks == 4 && j < 2) {
            stv[j] = ld16_sc1(rs, OFF_ST + (panel * 8 + 2 * w + j) * 1024 + lane * 16);
            g1[j] = ((const float*)(ws + OFF_G))[(panel * 128 + 32 * w + 16 * j + r16) * 2 + 1];
          }
          if (ks == 5) c4[j] = *(const f32x4*)(ws + OFF_CS + (size_t)(c14 * 128 + 16 * j + 4 * g) * 4);
          SB();
        }
      }
      ev_signal(smem, 3, w, lane);
      STAMP(6);
#pragma unroll
      for (int rf = 0; rf < 2; ++rf) {
        float s1 = __uint_as_float(stv[rf][0]), s2 = __uint_as_float(stv[rf][1]);
        if (g < 3) { s1 += __uint_as_float(stv[rf][2]); s2 += __uint_as_float(stv[rf][3]); }
        s1 = xsum4(s1); s2 = xsum4(s2);
        float mean = s1 * (1.f / D_);
        float var = fmaxf(s2 * (1.f / D_) - mean * mean, 0.f);
        float a_ = __builtin_amdgcn_rsqf(var + 1e-5f);
        float k1 = GC1 * a_, k3 = GC3 * a_ * a_ * a_, inv = __builtin_amdgcn_rcpf(fmaxf(a_ * g1[rf], 1e-30f));
        const f32x2 nm = {-mean, -mean};
#pragma unroll
        for (int jj = 0; jj < 4; ++jj) {
          u32x4 v;
#pragma unroll
          for (int h = 0; h < 2; ++h) {
            const int j = 2 * jj + h;
            f32x2 z0 = __builtin_elementwise_fma(nm, f32x2{c4[j][0], c4[j][1]}, f32x2{acc[rf][j][0], acc[rf][j][1]});
            f32x2 z1 = __builtin_elementwise_fma(nm, f32x2{c4[j][2], c4[j][3]}, f32x2{acc[rf][j][2], acc[rf][j][3]});
            v[2 * h] = pack2v(gelu2(z0, k1, k3, inv));
            v[2 * h + 1] = pack2v(gelu2(z1, k1, k3, inv));
          }
          st16_sc1(rs, OFF_AC + (((panel * 8 + 2 * w + rf) * 72 + 16 + c14 * 4 + jj) * 64 + lane) * 16, v);
        }
      }
    }
    STAMP(7);
    WAIT_V(0);
    STAMP(12);
    ev_signal(smem, 10, w, lane);
    if (w == 0) {
      ev_wait(smem, 10);
      post_flags(ws, 2, cons4, c14, lane);
    }
    __builtin_amdgcn_s_setprio(0);
    ev_wait(smem, 14);
#pragma unroll
    for (int m = 0; m < 7; ++m) {
      const int n = 7 * w + m;
      const char* sl = smem + (n < 27 ? n * 4096 : L_C) + lane * 16;
#pragma unroll
      for (int j = 0; j < 4; ++j) bp[m][j] = ld_frag(sl + ((j << 10) ^ ((w8 & 1) << 11)));
    }
    ev_signal(smem, 16, w, lane);
    STAMP(8);
    if (w == 0) {
      wait_flags(ws, 2, 14, lane);
      ev_signal_all(smem, 13, lane);
    }
  } else {
    const int gb = (7 * head) >> 1;
    const int qw = panel * 128 + 32 * w;
    BAR();
    __builtin_amdgcn_s_setprio(3);
    {
      const unsigned lv = (lane & 48) | ((lane & 15) ^ ((lane & 16) >> 1));
      const char* src = ws + OFF_QKV + (size_t)(56 + gb) * 256;
#pragma unroll
      for (int m = 0; m < 6; ++m) {
        const int lt = 6 * w + m;
        int kt = panel * 8 - 16 + lt;
        kt = kt < 0 ? 0 : kt;
        glds16(src + (size_t)kt * 43008, lane * 16, l0 + L_C + lt * 1024);
        glds16(src + (size_t)kt * 43008 + 56 * 256, lv * 16, l0 + L_C + 24576 + lt * 1024);
      }
    }
    u32x4 qv[2];
#pragma unroll
    for (int h = 0; h < 2; ++h)
      asm volatile("global_load_dwordx4 %0, %1, %2" : "=v"(qv[h]) : "v"((((panel * 8 + 2 * w + h) * 168 + gb) * 16 + lane) * 16), "s"(ws + OFF_QKV) : "memory");
    if (has_gemm) {
      const unsigned o0 = (88 * w + r8) * (F_ * 2) + ((w & 1) ? cO : cE), o1 = (88 * w + r8) * (F_ * 2) + ((w & 1) ? cE : cO);
      const unsigned ld = l0 + L_H1 + w * 11264;
#pragma unroll
      for (int m = 0; m < 11; ++m) glds16(w3b + 128, ((m & 1) ? o1 : o0) + m * 8 * F_ * 2, ld + m * 1024);
      WAIT_V(11);
    } else {
      WAIT_V(0);
    }
    asm volatile("" : "+v"(qv[0]), "+v"(qv[1])::"memory");
    ev_signal(smem, 4, w, lane);
    ev_wait(smem, 4);
    STAMP(1);
    const char* sK = smem + L_C;
    const char* sV = smem + L_C + 24576;
    const bool odd = head & 1;
    const bool z01 = odd && g == 0, z23 = !odd && g == 3;
    bf16x8 qf[2];
#pragma unroll
    for (int h = 0; h < 2; ++h) {
      qv[h][0] = z01 ? 0u : qv[h][0]; qv[h][1] = z01 ? 0u : qv[h][1];
      qv[h][2] = z23 ? 0u : qv[h][2]; qv[h][3] = z23 ? 0u : qv[h][3];
      qf[h] = as_frag(qv[h]);
    }
    const float NEG = -__builtin_inff();
    constexpr float SC = 0.1889822365046136f * 1.4426950408889634f;
    f32x4 s[2][17];
    float mx[2] = {NEG, NEG};
    auto max4 = [&](float m, const f32x4& v) {
      return __builtin_fmaxf(__builtin_fmaxf(__builtin_fmaxf(__builtin_fmaxf(m, v[0]), v[1]), v[2]), v[3]);
    };
    auto finish = [&](int h, int t) {
      if (t == 0 || t == 16) {
#pragma unroll
        for (int i = 0; i < 4; ++i) {
          const int b = 4 * g + i;
          s[h][t][i] = ((b > r16) == (t == 0)) ? s[h][t][i] : NEG;
        }
      }
      mx[h] = max4(mx[h], s[h][t]);
    };
    {
      bf16x8 kf[6];
#pragma unroll
      for (int t = 0; t < 6; ++t) kf[t] = ld_frag(sK + (2 * w + t) * 1024 + lane * 16);
      SB();
#pragma unroll
      for (int t = 0; t < 20; ++t) {
        if (t < 18) {
          f32x4 z = {};
          if (t < 17) s[0][t] = MFMA16(kf[t % 6], qf[0], z);
          if (t > 0) s[1][t - 1] = MFMA16(kf[t % 6], qf[1], z);
          if (t + 6 < 18) kf[t % 6] = ld_frag(sK + (2 * w + t + 6) * 1024 + lane * 16);
        }
        if (t >= 2) {
          if (t - 2 < 17) finish(0, t - 2);
          if (t - 2 > 0) finish(1, t - 3);
        }
        SB();
      }
    }
    if (qw < 256) {
#pragma unroll
      for (int h = 0; h < 2; ++h) {
        const int tstart = 16 - (qw >> 4) - h;
        mx[h] = NEG;
#pragma unroll
        for (int t = 0; t < 17; ++t) {
          if (t < tstart) s[h][t] = f32x4{NEG, NEG, NEG, NEG};
          mx[h] = max4(mx[h], s[h][t]);
        }
      }
    }
    const float mc[2] = {-xmax4(mx[0]) * SC, -xmax4(mx[1]) * SC};
    STAMP(2);
    WAIT_V(0);
    ev_signal(smem, 6, w, lane);
    float gt[2];
#pragma unroll
    for (int h = 0; h < 2; ++h) gt[h] = ((const float*)(ws + OFF_G))[(qw + 16 * h + r16) * 2];
    STAMP(3);
    f32x4 o[2][2] = {}, lsum[2] = {};
    bf16x8 ones;
#pragma unroll
    for (int i = 0; i < 8; ++i) ones[i] = (bf16)1.0f;
    auto pexp = [&](const f32x4& sv, float m, unsigned& lo, unsigned& hi) {
      f32x2 a = __builtin_elementwise_fma(f32x2{sv[0], sv[1]}, f32x2{SC, SC}, f32x2{m, m});
      f32x2 b = __builtin_elementwise_fma(f32x2{sv[2], sv[3]}, f32x2{SC, SC}, f32x2{m, m});
      lo = pack2(__builtin_amdgcn_exp2f(a[0]), __builtin_amdgcn_exp2f(a[1]));
      hi = pack2(__builtin_amdgcn_exp2f(b[0]), __builtin_amdgcn_exp2f(b[1]));
    };
    const int voff0 = (p >> 1) * 256 + (((4 * g + q) ^ ((p >> 1) << 3)) << 4) + (p & 1) * 8;
    bf16x8 vf[2][2], pf[2][2];
#pragma unroll
    for (int kk = 0; kk < 10; ++kk) {
      if (kk < 9) {
        const char* vb = sV + (2 * w + 2 * kk) * 1024 + voff0;
        vf[kk & 1][0] = ld_tr2(vb, vb + 1024);
        vf[kk & 1][1] = ld_tr2(vb + 512, vb + 1536);
#pragma unroll
        for (int h = 0; h < 2; ++h) {
          unsigned p0 = 0, p1 = 0, p2 = 0, p3 = 0;
          const int t0 = 2 * kk - h, t1 = t0 + 1;
          if (t0 >= 0) pexp(s[h][t0 < 0 ? 0 : t0], mc[h], p0, p1);
          if (t1 <= 16) pexp(s[h][t1 > 16 ? 16 : t1], mc[h], p2, p3);
          pf[kk & 1][h] = as_frag(u32x4{p0, p1, p2, p3});
        }
      }
      if (kk > 0) {
        const int c = (kk - 1) & 1;
#pragma unroll
        for (int h = 0; h < 2; ++h) {
          o[h][0] = MFMA16(vf[c][0], pf[c][h], o[h][0]);
          o[h][1] = MFMA16(vf[c][1], pf[c][h], o[h][1]);
          lsum[h] = MFMA16(ones, pf[c][h], lsum[h]);
        }
      }
      SGB(0x100, 4);
#pragma unroll
      for (int m = 0; m < 6; ++m) { SGB(0x8, 1); SGB(0x402, 5); }
      SB();
    }
    __builtin_amdgcn_s_setprio(3);
    ev_signal(smem, 5, w, lane);
    STAMP(4);
#pragma unroll
    for (int h = 0; h < 2; ++h) {
      float sc = gt[h] * __builtin_amdgcn_rcpf(lsum[h][0]);
      u32x4 v;
      v[0] = z01 ? 0u : pack2(o[h][0][0] * sc, o[h][0][1] * sc);
      v[1] = z01 ? 0u : pack2(o[h][0][2] * sc, o[h][0][3] * sc);
      v[2] = z23 ? 0u : pack2(o[h][1][0] * sc, o[h][1][1] * sc);
      v[3] = z23 ? 0u : pack2(o[h][1][2] * sc, o[h][1][3] * sc);
      st16_sc1(rs, OFF_AC + (((panel * 8 + 2 * w + h) * 72 + head) * 64 + lane) * 16, v);
    }
    if (!has_gemm) {
      WAIT_V(0);
      ev_signal(smem, 11, w, lane);
      if (w == 0) {
        ev_wait(smem, 11);
        post_flags(ws, 1, cons4, head, lane);
      }
      return;
    }
    ev_wait(smem, 5);
    glds16(w3b + 128 + (size_t)(416 + 8 * w) * F_ * 2, r8 * (F_ * 2) + ((w & 1) ? cO : cE), l0 + L_C + w * 1024);
  }
  {
    unsigned aoff[4];
#pragma unroll
    for (int rt = 0; rt < 4; ++rt) aoff[rt] = OFF_AC + (((rt64 * 4 + (rt ^ (w8 >> 1))) * 72 + w) * 64 + lane) * 16;
    f32x4 acc[4][4] = {};
    u32x4 a[3][4];
    auto ldA = [&](int buf, int k4) {
#pragma unroll
      for (int rt = 0; rt < 4; ++rt) a[buf][rt] = ld16_sc1(rs, aoff[rt] + k4 * 4096);
    };
    auto mma = [&](const u32x4 (&af)[4], const bf16x8 (&bf)[4]) {
#pragma unroll
      for (int rt = 0; rt < 4; ++rt)
#pragma unroll
        for (int j = 0; j < 4; ++j) acc[rt][j] = MFMA16(bf[j], as_frag(af[rt]), acc[rt][j]);
    };
    if (grp == 1) {
      bf16x8 bo[4];
      WAIT_V(0);
      ev_signal(smem, 7, w, lane);
      STAMP(5);
      if (w == 0) {
        ev_wait(smem, 7);
        post_flags(ws, 1, cons4, head, lane);
      }
      ev_wait(smem, 15);
      ldB(bp[6], 0); ldB(bo, 1);
#pragma unroll
      for (int m = 0; m < 6; ++m) ldB(bp[m], 4 + m);
      STAMP(6);
#pragma unroll
      for (int m = 0; m < 7; ++m)
#pragma unroll
        for (int j = 0; j < 4; ++j) asm volatile("" : "+v"(bp[m][j]));
#pragma unroll
      for (int j = 0; j < 4; ++j) asm volatile("" : "+v"(bo[j]));
      ev_wait(smem, 3);
#pragma unroll
      for (int m = 0; m < 7; ++m) {
        const int n = 7 * w + m;
        const unsigned dst = l0 + (n < 27 ? n * 4096 : L_C);
        const char* src = ws + OFF_BT + (size_t)(c7 * 72 + 44 + w + 4 * m) * 4096;
        glds16<0>(src, lane * 16, dst); glds16<1024>(src, lane * 16, dst);
        glds16<2048>(src, lane * 16, dst); glds16<3072>(src, lane * 16, dst);
        WAIT_V(8);
      }
      if (w == 0) {
        wait_flags(ws, 1, 16, lane);
        ev_signal_all(smem, 12, lane);
      } else {
        ev_wait(smem, 12);
      }
      ldA(0, 0); ldA(1, 1);
      WAIT_V(8);
      ev_signal(smem, 14, w, lane);
      STAMP(7);
      __builtin_amdgcn_s_setprio(0);
      SB();
      mma(a[0], bp[6]);
      SB();
      ldB(bp[6], 2); ldA(0, 2);
      SB();
      mma(a[1], bo);
      SB();
      ldB(bo, 3); ldA(1, 3);
      SB();
      mma(a[0], bp[6]);
      SB();
      ldB(bp[6], 10);
      SB();
      mma(a[1], bo);
      SB();
      STAMP(8);
    }
    ev_wait(smem, 13);
    STAMP(9);
    const int k0 = grp ? 4 : 11;
#pragma unroll
    for (int m = 0; m < 3; ++m) ldA(m, k0 + m);
    const int tt = rt64 * 4 + (w8 >> 1);
    u32x4 x1v;
    float g1o;
    SB();
#pragma unroll
    for (int m = 0; m < 7; ++m) {
      mma(a[m % 3], bp[m]);
      SB();
      if (m + 3 < 7) ldA(m % 3, k0 + m + 3);
      if (m == 4) {
        x1v = ld16_sc1(rs, OFF_X1 + ((tt * 14 + 2 * c7 + (w8 & 1)) * 64 + lane) * 16);
        g1o = ((const float*)(ws + OFF_G))[(tt * 16 + r16) * 2 + 1];
      }
      SB();
    }
    STAMP(10);
    ev_wait(smem, 16);
    auto exa = [&](int dst) { return (f32x4*)(smem + dst * 14336 + (dst == 7 ? L_C - 7 * 14336 : 0)) + lane; };
#pragma unroll
    for (int pos = 1; pos < 8; ++pos)
#pragma unroll
      for (int h = 0; h < 2; ++h) exa(w8 ^ pos)[((pos - 1) * 2 + h) * 64] = acc[pos >> 1][2 * (pos & 1) + h];
    BAR();
    STAMP(13);
    unsigned pk[2][2];
#pragma unroll
    for (int h = 0; h < 2; ++h) {
      f32x4 r = acc[0][h];
#pragma unroll
      for (int sidx = 0; sidx < 7; ++sidx) r += exa(w8)[(sidx * 2 + h) * 64];
      pk[h][0] = pack2(__builtin_fmaf(g1o, bf_lo(x1v[2 * h]), r[0]), __builtin_fmaf(g1o, bf_hi(x1v[2 * h]), r[1]));
      pk[h][1] = pack2(__builtin_fmaf(g1o, bf_lo(x1v[2 * h + 1]), r[2]), __builtin_fmaf(g1o, bf_hi(x1v[2 * h + 1]), r[3]));
    }
    auto s0 = __builtin_amdgcn_permlane16_swap(pk[0][0], pk[1][0], false, false);
    auto s1 = __builtin_amdgcn_permlane16_swap(pk[0][1], pk[1][1], false, false);
    u32x4 v = {s0[0], s1[0], s0[1], s1[1]};
    const int gl = (g >> 1) | ((g & 1) << 1);
    *(u32x4*)(out + (size_t)(tt * 16 + r16) * D_ + c7 * 64 + 32 * (w8 & 1) + 8 * gl) = v;
  }
  STAMP_FLUSH(ws, 1);
}

extern "C" void kernel_launch(void* const* d_in, const int* in_sizes, int n_in, void* d_out, int out_size, void* d_ws,
                              size_t ws_size, hipStream_t stream) {
  const bf16* x = (const bf16*)d_in[0];
  const bf16* wqkv = (const bf16*)d_in[1];
  const bf16* wo = (const bf16*)d_in[2];
  const bf16* w1 = (const bf16*)d_in[4];
  const bf16* w2 = (const bf16*)d_in[5];
  const bf16* w3 = (const bf16*)d_in[6];
  const bf16* w4 = (const bf16*)d_in[7];
  const bf16* gw = (const bf16*)d_in[8];
  char* ws = (char*)d_ws;
  k1_qkv_h<<<256, 384, 0, stream>>>(x, wqkv, w1, w2, w3, w4, wo, gw, ws);
  k2_mega<<<256, 512, 0, stream>>>(x, w2, w3, w4, wo, ws, (bf16*)d_out);
}
```

```cpp
#include <hip/hip_runtime.h>
#include <hip/hip_bf16.h>

typedef __bf16 bf16;
typedef __bf16 bf16x2 __attribute__((ext_vector_type(2)));
typedef __bf16 bf16x4 __attribute__((ext_vector_type(4)));
typedef __bf16 bf16x8 __attribute__((ext_vector_type(8)));
typedef float f32x2 __attribute__((ext_vector_type(2)));
typedef float f32x4 __attribute__((ext_vector_type(4)));
typedef unsigned u32x2 __attribute__((ext_vector_type(2)));
typedef unsigned u32x4 __attribute__((ext_vector_type(4)));
#define LDS_AS __attribute__((address_space(3)))
#define DI __device__ __forceinline__

constexpr int S_ = 2048, D_ = 448, F_ = 1792;
constexpr size_t OFF_QKV = 0;
constexpr size_t OFF_HS = OFF_QKV + 5505024;
constexpr size_t OFF_X1 = OFF_HS + 1835008;
constexpr size_t OFF_AC = OFF_X1 + 1835008;
constexpr size_t OFF_ST = OFF_AC + 9437184;
constexpr size_t OFF_G  = OFF_ST + 262144;
constexpr size_t OFF_CS = OFF_G + 16384;
constexpr size_t OFF_FL = OFF_CS + 8192;
constexpr size_t OFF_BT = OFF_FL + 256 * 8 * 128;
constexpr size_t WS_USED = OFF_BT + 7 * 72 * 4096;

#define STAMP_DECL
#define STAMP(i)
#define STAMP_FLUSH(ws, kid)
#define STAMP_ARG
#define STAMP_PASS

#define WAIT_V(n) asm volatile("s_waitcnt vmcnt(" #n ")" ::: "memory")
#define BAR() do { asm volatile("s_waitcnt lgkmcnt(0)" ::: "memory"); __builtin_amdgcn_s_barrier(); asm volatile("" ::: "memory"); } while (0)
#define MFMA16(a, b, c) __builtin_amdgcn_mfma_f32_16x16x32_bf16((a), (b), (c), 0, 0, 0)

template <int IMM = 0>
DI void glds16(const void* sbase, unsigned voff, unsigned lds) {
  asm volatile("s_mov_b32 m0, %2\n\ts_nop 0\n\tglobal_load_lds_dwordx4 %0, %1 offset:%3" ::"v"(voff), "s"(sbase), "s"(lds), "i"(IMM) : "memory");
}
template <int LOFF>
DI void glds16c(const void* sbase, unsigned voff, unsigned ldsbase) {
  asm volatile("s_add_u32 m0, %2, %3\n\ts_nop 0\n\tglobal_load_lds_dwordx4 %0, %1" ::"v"(voff), "s"(sbase), "s"(ldsbase), "i"(LOFF) : "memory", "scc");
}
DI void touch8k(unsigned& sink, const void* sbase, unsigned chunk, int lane) {
  asm volatile("global_load_dword %0, %1, %2" : "+v"(sink) : "v"(chunk * 8192u + lane * 128u), "s"(sbase) : "memory");
}
DI void touch_keep(unsigned& sink) { asm volatile("" : "+v"(sink)::"memory"); }
DI unsigned lds_addr(const char* p) { return (unsigned)(size_t)(const LDS_AS char*)p; }
DI bf16x8 ld_frag(const char* lds) { return *(const bf16x8*)lds; }
DI bf16x8 ld_tr2(const char* p0, const char* p1) {
  bf16x4 a = __builtin_amdgcn_ds_read_tr16_b64_v4bf16((LDS_AS bf16x4*)p0);
  bf16x4 b = __builtin_amdgcn_ds_read_tr16_b64_v4bf16((LDS_AS bf16x4*)p1);
  return __builtin_shufflevector(a, b, 0, 1, 2, 3, 4, 5, 6, 7);
}
DI unsigned pack2(float a, float b) {
  f32x2 t = {a, b};
  return __builtin_bit_cast(unsigned, __builtin_convertvector(t, bf16x2));
}
DI float bf_lo(unsigned u) { return __builtin_bit_cast(float, u << 16); }
DI float bf_hi(unsigned u) { return __builtin_bit_cast(float, u & 0xffff0000u); }
DI float xsum4(float x) {
  auto a = __builtin_amdgcn_permlane16_swap(__float_as_uint(x), __float_as_uint(x), false, false);
  x = __uint_as_float(a[0]) + __uint_as_float(a[1]);
  auto b = __builtin_amdgcn_permlane32_swap(__float_as_uint(x), __float_as_uint(x), false, false);
  return __uint_as_float(b[0]) + __uint_as_float(b[1]);
}
DI float xmax4(float x) {
  auto a = __builtin_amdgcn_permlane16_swap(__float_as_uint(x), __float_as_uint(x), false, false);
  x = fmaxf(__uint_as_float(a[0]), __uint_as_float(a[1]));
  auto b = __builtin_amdgcn_permlane32_swap(__float_as_uint(x), __float_as_uint(x), false, false);
  return fmaxf(__uint_as_float(b[0]), __uint_as_float(b[1]));
}

DI float gelu_ln(float z, float k1, float k3, float inv) {
  float t = z * z;
  float w = __builtin_fmaf(t, k3, k1);
  float e = __builtin_amdgcn_exp2f(z * w);
  float d = __builtin_fmaf(e, inv, inv);
  return z * __builtin_amdgcn_rcpf(d);
}
constexpr float GC1 = -2.0f * 0.7978845608028654f * 1.4426950408889634f;
constexpr float GC3 = GC1 * 0.044715f;

DI int swz(int row) { return ((row >> 1) & 1) | ((((row >> 2) ^ (row >> 3)) & 1) << 1); }
DI int src_chunk(int row, int lane) { return ((((lane & 7) >> 1) ^ swz(row)) << 1) | (lane & 1); }

template <int N> DI void wait_v() { asm volatile("s_waitcnt vmcnt(%0)" ::"i"(N) : "memory"); }
#define SB() __builtin_amdgcn_sched_barrier(0)
#define SGB(mask, n) __builtin_amdgcn_sched_group_barrier(mask, n, 0)
DI f32x2 gelu2(f32x2 z, float k1, float k3, float inv) {
  f32x2 t = z * z;
  f32x2 w = __builtin_elementwise_fma(t, f32x2{k3, k3}, f32x2{k1, k1});
  f32x2 u = z * w;
  f32x2 e = {__builtin_amdgcn_exp2f(u[0]), __builtin_amdgcn_exp2f(u[1])};
  f32x2 d = __builtin_elementwise_fma(e, f32x2{inv, inv}, f32x2{inv, inv});
  f32x2 r = {__builtin_amdgcn_rcpf(d[0]), __builtin_amdgcn_rcpf(d[1])};
  return z * r;
}
DI unsigned pack2v(f32x2 t) { return __builtin_bit_cast(unsigned, __builtin_convertvector(t, bf16x2)); }

#define CODE_ADDR(p, sym) do { int off_; asm volatile("s_getpc_b64 %0\n\ts_mov_b32 %1, " #sym "@rel32@lo+4" : "=s"(p), "=s"(off_)); p += off_; } while (0)
constexpr unsigned K2_CODE_BYTES = 26000;

#define NTOUCH 5
constexpr int K1_A = 114688;
template <bool STATS>
DI void k1_compute(char* smem, const char* xrows, int w, int lane, f32x4 (&acc)[2][8], f32x4& cs, f32x4 (&sum)[2],
                   f32x4 (&sq)[2] STAMP_ARG) {
  const int g = lane >> 4, q = (lane >> 2) & 3, p = lane & 3, r16 = lane & 15;
  const char* sa = smem + K1_A + w * 12288;
  const unsigned la = lds_addr(smem) + K1_A + w * 12288;
  const unsigned xoff = ((32 * w + (lane >> 3)) * D_ + (((lane & 7) ^ (lane >> 3)) << 3)) * 2;
  auto issueA = [&](int st, int slot) {
#pragma unroll
    for (int i = 0; i < 4; ++i) glds16(xrows + st * 128, xoff + i * 8 * D_ * 2, la + slot * 4096 + i * 1024);
  };
  issueA(0, 0); issueA(1, 1); issueA(2, 2);
  const int a0 = r16 * 128 + ((g ^ (r16 & 7)) << 4);
  const int b0 = (8 * g + q) * 128 + (swz(8 * g + q) << 5) + p * 8;
  const int b1 = (8 * g + 4 + q) * 128 + (swz(8 * g + 4 + q) << 5) + p * 8;
  bf16x8 ones;
#pragma unroll
  for (int i = 0; i < 8; ++i) ones[i] = (bf16)1.0f;
  bf16x8 A[2][2], B[2][8], Bw[2];
  auto rdA = [&](const char* abase, const char* bbase, int u) {
    A[u][0] = ld_frag(abase + (a0 ^ (u << 6)));
    A[u][1] = ld_frag(abase + (a0 ^ (u << 6)) + 2048);
    if (STATS) Bw[u] = ld_tr2(bbase + 57344 + (b0 ^ (w << 5)), bbase + 57344 + (b1 ^ (w << 5)));
  };
  auto rdB = [&](const char* bbase, int u, int j) {
    const int o = (j >> 2) * 57344;
    B[u][j] = ld_tr2(bbase + o + (b0 ^ ((j & 3) << 5)), bbase + o + (b1 ^ ((j & 3) << 5)));
  };
  auto kstep = [&](int u, const char* nabase, const char* nbbase, bool more) {
#pragma unroll
    for (int j = 0; j < 8; ++j) {
      acc[0][j] = MFMA16(B[u][j], A[u][0], acc[0][j]);
      acc[1][j] = MFMA16(B[u][j], A[u][1], acc[1][j]);
      if (more) { if (j == 0) rdA(nabase, nbbase, u ^ 1); rdB(nbbase, u ^ 1, j); }
      if (STATS) {
        if (j == 1) sum[0] = MFMA16(ones, A[u][0], sum[0]);
        if (j == 2) sum[1] = MFMA16(ones, A[u][1], sum[1]);
        if (j == 4) sq[0] = MFMA16(A[u][0], A[u][0], sq[0]);
        if (j == 5) sq[1] = MFMA16(A[u][1], A[u][1], sq[1]);
        if (j == 7) cs = MFMA16(Bw[u], ones, cs);
      }
      SB();
    }
  };
  STAMP(1);
  WAIT_V(8);
  BAR(); STAMP(2);
  rdA(sa, smem, 0);
#pragma unroll
  for (int j = 0; j < 8; ++j) rdB(smem, 0, j);
  SB();
#pragma unroll
  for (int s = 0; s < 6; ++s) {
    kstep(0, sa + (s % 3) * 4096, smem + s * 8192 + 4096, true);
    if (s < 5) WAIT_V(4); else WAIT_V(0);
    if (s < 4) asm volatile("s_waitcnt lgkmcnt(14)" ::: "memory");
    if (s < 2 || s == 3) __builtin_amdgcn_s_barrier();
    asm volatile("" ::: "memory");
    if (s < 4) issueA(s + 3, s % 3);
    kstep(1, sa + ((s + 1) % 3) * 4096, smem + s * 8192 + 8192, true);
  }
  kstep(0, sa, smem + 6 * 8192 + 4096, true);
  kstep(1, sa, smem, false);
  STAMP(9);
}

extern "C" __global__ __launch_bounds__(384, 2) void k1_qkv_h(const bf16* __restrict__ x, const bf16* __restrict__ wqkv,
                                                   const bf16* __restrict__ w1, const bf16* __restrict__ w2,
                                                   const bf16* __restrict__ w3, const bf16* __restrict__ w4,
                                                   const bf16* __restrict__ wo, const bf16* __restrict__ gw,
                                                   char* __restrict__ ws) {
  __shared__ __attribute__((aligned(16))) char smem[163840];
  const int tid = threadIdx.x, lane = tid & 63;
  const int w = __builtin_amdgcn_readfirstlane(tid >> 6);
  const int xcd = blockIdx.x & 7, idx = blockIdx.x >> 3;
  STAMP_DECL STAMP(0);
  if (idx >= 28) {
    const int hb = xcd * 4 + (idx - 28);
    float* red = (float*)smem;
    if (tid < 256) {
      ((u32x4*)(ws + OFF_FL))[hb * 512 + tid] = u32x4{0u, 0u, 0u, 0u};
      ((u32x4*)(ws + OFF_FL))[hb * 512 + 256 + tid] = u32x4{0u, 0u, 0u, 0u};
    }
    if (w < 4) {
      const int r8 = lane >> 3;
      const unsigned cE = src_chunk(r8, lane) * 16, cO = src_chunk(r8 + 8, lane) * 16;
      const unsigned lt = lds_addr(smem) + 4096 + w * 16384;
#pragma unroll
      for (int m = 0; m < 4; ++m) {
        int u = hb * 16 + w + 4 * m;
        u = u < 504 ? u : 503;
        const int c7 = u / 72, k = u - c7 * 72;
        const char* src = k < 16 ? (const char*)(wo + (size_t)k * 28 * D_ + c7 * 64) : (const char*)(w4 + (size_t)(k - 16) * 32 * D_ + c7 * 64);
#pragma unroll
        for (int i = 0; i < 4; ++i) {
          int d = 8 * i + r8;
          if (k < 16) { d -= 4 * (k & 1); d = d < 0 ? 0 : (d > 27 ? 27 : d); }
          glds16(src, d * (D_ * 2) + ((i & 1) ? cO : cE), lt + m * 4096 + i * 1024);
        }
      }
      if (hb < 28) {
        float a[8] = {};
#pragma unroll
        for (int i = 0; i < 14; ++i) {
          bf16x8 v = *(const bf16x8*)(w3 + (size_t)(w * 112 + i * 8 + (lane >> 3)) * F_ + hb * 64 + (lane & 7) * 8);
#pragma unroll
          for (int e = 0; e < 8; ++e) a[e] += (float)v[e];
        }
#pragma unroll
        for (int e = 0; e < 8; ++e) {
          a[e] += __shfl_xor(a[e], 8);
          a[e] = xsum4(a[e]);
        }
        if (lane < 8) {
#pragma unroll
          for (int e = 0; e < 8; ++e) red[w * 64 + lane * 8 + e] = a[e];
        }
      }
      WAIT_V(0);
      const int g = lane >> 4, q = (lane >> 2) & 3;
      const int btr = (4 * g + q) * 128 + (swz(4 * g + q) << 5) + (lane & 3) * 8;
#pragma unroll
      for (int m = 0; m < 4; ++m) {
        const int u = hb * 16 + w + 4 * m;
        if (u < 504) {
#pragma unroll
          for (int j = 0; j < 4; ++j) {
            const char* bp = smem + 4096 + w * 16384 + m * 4096 + (btr ^ (j << 5));
            *(bf16x8*)(ws + OFF_BT + (size_t)u * 4096 + j * 1024 + lane * 16) = ld_tr2(bp, bp + 2048);
          }
        }
      }
    } else {
      bf16x8 gv = {};
      if (lane < 56) gv = *(const bf16x8*)(gw + lane * 8);
      float* G = (float*)(ws + OFF_G);
      {
        bf16x8 xv[32];
        const int row0 = hb * 64 + (w - 4) * 32;
#pragma unroll
        for (int r = 0; r < 32; ++r) {
          xv[r] = *(const bf16x8*)(x + (row0 + r) * D_ + (lane < 56 ? lane : 0) * 8);
        }
        float mine = 0.f;
#pragma unroll
        for (int r = 0; r < 32; ++r) {
          float s = 0.f;
#pragma unroll
          for (int i = 0; i < 4; ++i) {
            bf16x2 a = {xv[r][2 * i], xv[r][2 * i + 1]}, b = {gv[2 * i], gv[2 * i + 1]};
            s = __builtin_amdgcn_fdot2_f32_bf16(a, b, s, false);
          }
#pragma unroll
          for (int o = 8; o; o >>= 1) s += __shfl_xor(s, o);
          s = xsum4(s);
          mine = lane == r ? s : mine;
        }
        if (lane < 32) {
          f32x2 gg = {1.f / (1.f + __expf(-mine)), 1.f / (1.f + __expf(mine))};
          *(f32x2*)(G + (row0 + lane) * 2) = gg;
        }
      }
      {
        unsigned sink = 0;
        const char* code;
        CODE_ADDR(code, k2_mega);
        const unsigned off = min((unsigned)((((idx & 1) * 2 + w - 4) * 64 + lane) * 128), K2_CODE_BYTES);
        asm volatile("global_load_dword %0, %1, %2" : "+v"(sink) : "v"(off), "s"(code) : "memory");
        WAIT_V(0); touch_keep(sink);
      }
    }
    BAR();
    if (hb < 28 && tid < 64) ((float*)(ws + OFF_CS))[hb * 64 + tid] = (red[tid] + red[64 + tid]) + (red[128 + tid] + red[192 + tid]);
    STAMP_FLUSH(ws, 0);
    return;
  }
  const int panel = 4 * (xcd >> 1) + (idx & 3), ct = 7 * (xcd & 1) + (idx >> 2);
  const bool mixed = ct < 7;
  const int u0 = mixed ? ct : 2 * ct - 7;
  if (w >= 4) {
    const int r = lane >> 3;
    const bool second = w == 5;
    const unsigned l0 = lds_addr(smem) + (second ? 57344 : 0);
    const char* base = (second && mixed) ? (const char*)(w1 + ct * 64) : (const char*)(wqkv + (u0 + (second ? 1 : 0)) * 64);
    const int ld = (second && mixed) ? 896 : 2688;
    const unsigned cE = src_chunk(r, lane) * 16, cO = src_chunk(r + 8, lane) * 16;
    auto issue = [&](int st) {
#pragma unroll
      for (int i = 0; i < 8; ++i) glds16(base + (size_t)st * 64 * ld, (8 * i + r) * ld + ((i & 1) ? cO : cE), l0 + st * 8192 + i * 1024);
    };
    unsigned sink = 0;
#pragma unroll 1
    for (int st = 0; st < 7; ++st) {
      if (st == 2) {
        const char* code;
        CODE_ADDR(code, k1_qkv_h);
        const int c = xcd * 28 + idx;
        const char* pb = second ? code : c < 105 ? (const char*)(wqkv + 128 * 1344) : (const char*)(w1 + 128 * D_);
        touch8k(sink, pb, second ? (idx % 3) : c < 105 ? c : c < 140 ? c - 105 : 0, lane);
        touch8k(sink, (const char*)(x + (size_t)panel * 128 * D_), (idx >> 2) * 2 + (w - 4), lane);
        WAIT_V(10); BAR();
      }
      if (st == 5) { WAIT_V(26); BAR(); }
      issue(st);
    }
    WAIT_V(24); BAR();
    WAIT_V(0); BAR();
    __builtin_amdgcn_s_sleep(4);
#pragma unroll
    for (int m = 0; m < NTOUCH; ++m) {
      const int c = idx * 2 + (w - 4) + 56 * m;
      touch8k(sink, c < 49 ? (const char*)w2 : (const char*)w3, c < 49 ? c : c < 245 ? c - 49 : 0, lane);
    }
    if (mixed) BAR();
    WAIT_V(0);
    touch_keep(sink);
    return;
  }
  const int g = lane >> 4, r16 = lane & 15;
  f32x4 acc[2][8] = {};
  f32x4 cs = {};
  f32x4 sum[2] = {}, sq[2] = {};
  float* ex = (float*)smem;
  const char* xrows = (const char*)(x + (size_t)panel * 128 * D_);
  if (mixed) {
    k1_compute<true>(smem, xrows, w, lane, acc, cs, sum, sq STAMP_PASS);
    if (r16 == 0) *(f32x4*)(ex + w * 16 + 4 * g) = cs;
  } else {
    k1_compute<false>(smem, xrows, w, lane, acc, cs, sum, sq STAMP_PASS);
  }
  {
    const int gl = (g >> 1) | ((g & 1) << 1);
#pragma unroll
    for (int jj = 0; jj < 4; ++jj) {
      if (jj < 2 || !mixed) {
#pragma unroll
        for (int rf = 0; rf < 2; ++rf) {
          const int tt = panel * 8 + 2 * w + rf;
          unsigned a0 = pack2(acc[rf][2 * jj][0], acc[rf][2 * jj][1]), a1 = pack2(acc[rf][2 * jj][2], acc[rf][2 * jj][3]);
          unsigned b0 = pack2(acc[rf][2 * jj + 1][0], acc[rf][2 * jj + 1][1]), b1 = pack2(acc[rf][2 * jj + 1][2], acc[rf][2 * jj + 1][3]);
          auto s0 = __builtin_amdgcn_permlane16_swap(a0, b0, false, false);
          auto s1 = __builtin_amdgcn_permlane16_swap(a1, b1, false, false);
          u32x4 v = {s0[0], s1[0], s0[1], s1[1]};
          *(u32x4*)(ws + OFF_QKV + ((size_t)((tt * 168 + u0 * 8 + 4 * jj + gl) * 16 + r16) << 4)) = v;
        }
      }
    }
  }
  if (mixed) {
    BAR();
    const int nb = ct * 64;
    f32x4 c4[4];
#pragma unroll
    for (int j = 0; j < 4; ++j) c4[j] = *(const f32x4*)(ex + j * 16 + 4 * g);
    unsigned hv[2][4][2];
#pragma unroll
    for (int rf = 0; rf < 2; ++rf) {
      float mean = sum[rf][0] * (1.f / D_);
      float dg = (r16 & 2) ? ((r16 & 1) ? sq[rf][3] : sq[rf][2]) : ((r16 & 1) ? sq[rf][1] : sq[rf][0]);
      dg = xsum4(g == (r16 >> 2) ? dg : 0.f);
      float var = fmaxf(dg * (1.f / D_) - mean * mean, 0.f);
      float a = __builtin_amdgcn_rsqf(var + 1e-5f);
      float k1 = GC1 * a, k3 = GC3 * a * a * a, inv = __builtin_amdgcn_rcpf(a);
      const f32x2 nm = {-mean, -mean};
#pragma unroll
      for (int j = 0; j < 4; ++j) {
        f32x2 z0 = __builtin_elementwise_fma(nm, f32x2{c4[j][0], c4[j][1]}, f32x2{acc[rf][4 + j][0], acc[rf][4 + j][1]});
        f32x2 z1 = __builtin_elementwise_fma(nm, f32x2{c4[j][2], c4[j][3]}, f32x2{acc[rf][4 + j][2], acc[rf][4 + j][3]});
        hv[rf][j][0] = pack2v(gelu2(z0, k1, k3, inv));
        hv[rf][j][1] = pack2v(gelu2(z1, k1, k3, inv));
      }
    }
#pragma unroll
    for (int rf = 0; rf < 2; ++rf)
#pragma unroll
      for (int j = 0; j < 4; ++j) asm volatile("" : "+v"(hv[rf][j][0]), "+v"(hv[rf][j][1]));
#pragma unroll
    for (int jj = 0; jj < 2; ++jj) {
      const int ks = (nb >> 5) + jj;
      int sh[2];
#pragma unroll
      for (int h = 0; h < 2; ++h) {
        const int grp = (nb + 32 * jj + 16 * h) / 112;
        sh[h] = (grp & 2 ? 2 : 1) * (grp & 1 ? -1 : 1);
      }
#pragma unroll
      for (int rf = 0; rf < 2; ++rf) {
        const int tok = panel * 128 + 32 * w + 16 * rf + r16;
        int t2[2];
        u32x2 v[2];
#pragma unroll
        for (int h = 0; h < 2; ++h) {
          t2[h] = tok + sh[h];
          const bool ok = (unsigned)t2[h] < (unsigned)S_;
          t2[h] &= S_ - 1;
          v[h][0] = ok ? hv[rf][2 * jj + h][0] : 0u;
          v[h][1] = ok ? hv[rf][2 * jj + h][1] : 0u;
        }
        char* d0 = ws + OFF_HS + ((size_t)(((t2[0] >> 4) * 14 + ks) * 64 + g * 16 + (t2[0] & 15)) << 4);
        if (ks != 3 && ks != 10) {
          u32x4 vv = {v[0][0], v[0][1], v[1][0], v[1][1]};
          *(u32x4*)d0 = vv;
        } else {
          char* d1 = ws + OFF_HS + ((size_t)(((t2[1] >> 4) * 14 + ks) * 64 + g * 16 + (t2[1] & 15)) << 4) + 8;
          *(u32x2*)d0 = v[0];
          *(u32x2*)d1 = v[1];
        }
      }
    }
  }
  STAMP(10);
  STAMP_FLUSH(ws, 0);
}

typedef __attribute__((address_space(1))) unsigned gu32;
typedef __amdgpu_buffer_rsrc_t rsrc_t;
DI u32x4 ld16_sc1(rsrc_t r, unsigned off) { return __builtin_amdgcn_raw_buffer_load_b128(r, off, 0, 16); }
DI u32x4 ld16(rsrc_t r, unsigned off) { return __builtin_amdgcn_raw_buffer_load_b128(r, off, 0, 0); }
DI void st16_sc1(rsrc_t r, unsigned off, u32x4 v) { __builtin_amdgcn_raw_buffer_store_b128(v, r, off, 0, 16); }
DI void st8_sc1(rsrc_t r, unsigned off, u32x2 v) { __builtin_amdgcn_raw_buffer_store_b64(v, r, off, 0, 16); }
DI bf16x8 as_frag(u32x4 v) { return __builtin_bit_cast(bf16x8, v); }
DI void post_flags(char* ws, int box, int consumer, int n, int word, int lane) {
  if (lane < n)
    __hip_atomic_store((gu32*)(ws + OFF_FL) + (consumer * 8 + box) * 32 + word, 1u, __ATOMIC_RELAXED, __HIP_MEMORY_SCOPE_AGENT);
}
DI void wait_flags(char* ws, int box, int nwords, int lane) {
  gu32* f = (gu32*)(ws + OFF_FL) + (blockIdx.x * 8 + box) * 32 + (lane < nwords ? lane : 0);
#pragma unroll 1
  for (int spin = 0; spin < (1 << 18); ++spin) {
    unsigned v = __hip_atomic_load(f, __ATOMIC_RELAXED, __HIP_MEMORY_SCOPE_AGENT);
    if (__all(v == 1u)) break;
    __builtin_amdgcn_s_sleep(2);
  }
  asm volatile("" ::: "memory");
}
constexpr int L_H1 = 57344, L_X = 102400, L_EV = 110592, L_C = 114688;
DI void ev_signal(char* smem, int ev, int wq, int lane) {
  asm volatile("s_waitcnt lgkmcnt(0)" ::: "memory");
  if (lane == 0) *(volatile LDS_AS unsigned*)(LDS_AS char*)(smem + L_EV + ev * 16 + wq * 4) = 1u;
  asm volatile("" ::: "memory");
}
DI void ev_wait(char* smem, int ev) {
  asm volatile("" ::: "memory");
#pragma unroll 1
  for (int spin = 0; spin < (1 << 22); ++spin) {
    u32x4 v = *(volatile LDS_AS u32x4*)(LDS_AS char*)(smem + L_EV + ev * 16);
    if (__builtin_amdgcn_readfirstlane(v[0] & v[1] & v[2] & v[3])) break;
    __builtin_amdgcn_s_sleep(2);
  }
  asm volatile("" ::: "memory");
}
DI void ev_signal_all(char* smem, int ev, int lane) {
  asm volatile("" ::: "memory");
  if (lane < 4) *(volatile LDS_AS unsigned*)(LDS_AS char*)(smem + L_EV + ev * 16 + lane * 4) = 1u;
  asm volatile("" ::: "memory");
}
extern "C" __global__ __launch_bounds__(512, 2) void k2_mega(const bf16* __restrict__ x, const bf16* __restrict__ w2,
                                                  const bf16* __restrict__ w3, const bf16* __restrict__ w4,
                                                  const bf16* __restrict__ wo, char* __restrict__ ws,
                                                  bf16* __restrict__ out) {
  __shared__ __attribute__((aligned(16))) char smem[163840];
  const int tid = threadIdx.x, lane = tid & 63;
  const int w8 = __builtin_amdgcn_readfirstlane(tid >> 6);
  const int grp = w8 >> 2, w = w8 & 3;
  const int xcd = blockIdx.x & 7, idx = blockIdx.x >> 3;
  STAMP_DECL STAMP(0);
  const unsigned l0 = lds_addr(smem);
  const bool has_gemm = idx < 28;
  const int rt64 = 4 * xcd + (idx & 3), c7 = idx >> 2;
  const int panel = 2 * xcd + (idx & 1), c14 = idx >> 1;
  const int qb = idx >> 1, head = 2 * xcd + (idx & 1);
  const int g = lane >> 4, q = (lane >> 2) & 3, p = lane & 3, r16 = lane & 15, r8 = lane >> 3;
  const rsrc_t rs = __builtin_amdgcn_make_buffer_rsrc(ws, 0, (int)WS_USED, 0x00020000);
  const int btr = (4 * g + q) * 128 + (swz(4 * g + q) << 5) + p * 8;
  const unsigned cE = src_chunk(r8, lane) * 16, cO = src_chunk(r8 + 8, lane) * 16;
  auto loadW = [&](const char* base, int ld, int g0, int n, unsigned dst) {
    const unsigned oE = r8 * ld + cE, oO = (r8 + 8) * ld + cO;
#pragma unroll 1
    for (int m = g0; m < g0 + n; ++m) {
      glds16(base + (size_t)m * 16 * ld, oE, dst + m * 2048);
      glds16(base + (size_t)m * 16 * ld, oO, dst + m * 2048 + 1024);
    }
  };
  const char* w3b = (const char*)(w3 + c14 * 128);
  bf16x8 bp[7][4];
  const unsigned boff = OFF_BT + (c7 * 72 + w) * 4096 + lane * 16;
  auto ldB = [&](bf16x8 (&bf)[4], int k4) {
#pragma unroll
    for (int j = 0; j < 4; ++j) bf[j] = as_frag(ld16(rs, (boff ^ ((w8 & 1) << 11) ^ (j << 10)) + k4 * 16384));
  };
  if (tid < 128) ((unsigned*)(smem + L_EV))[tid] = 0u;
  const int cons3 = ((2 * lane + ((rt64 >> 1) & 1)) << 3) | xcd;
  const int cons4 = ((4 * (lane >> 1) + 2 * (qb & 1) + (lane & 1)) << 3) | (qb >> 1);

  if (grp == 0) {
    if (!has_gemm) {
      BAR();
      unsigned sink = 0;
      const int wv = (idx - 28) * 4 + w;
      __builtin_amdgcn_s_sleep(110);
#pragma unroll 1
      for (int m = 0; m < 16; ++m) {
        const int c = wv + 16 * m;
        touch8k(sink, ws + OFF_BT, c < 252 ? c : 0, lane);
        WAIT_V(1);
      }
      WAIT_V(0); touch_keep(sink);
      return;
    }
    __builtin_amdgcn_s_setprio(2);
    loadW((const char*)(w2 + c7 * 64), D_ * 2, 7 * w, 7, l0);
    {
      const char* bx = (const char*)(x + (size_t)(rt64 * 64 + 16 * w) * D_ + c7 * 64);
#pragma unroll
      for (int i = 0; i < 2; ++i) glds16(bx, ((8 * i + r8) * D_ + (((lane & 7) ^ r8) << 3)) * 2, l0 + L_X + w * 2048 + i * 1024);
    }
    u32x4 ha[14];
#pragma unroll
    for (int ks = 0; ks < 14; ++ks) ha[ks] = ld16(rs, OFF_HS + (((rt64 * 4 + w) * 14 + ks) * 64 + lane) * 16);
    BAR();
    WAIT_V(14);
    ev_signal(smem, 0, w, lane);
    ev_wait(smem, 0);
    STAMP(1);
    {
      f32x4 acc[4] = {};
      bf16x8 bq[3][4];
      auto rdB = [&](int ks, int j) {
        const char* bp = smem + ks * 4096 + (btr ^ (j << 5));
        bq[ks % 3][j] = ld_tr2(bp, bp + 2048);
      };
#pragma unroll
      for (int j = 0; j < 4; ++j) { rdB(0, j); rdB(1, j); }
      const int trow = 16 * w + r16;
      u32x2 xr[4];
      SB();
#pragma unroll
      for (int ks = 0; ks < 14; ++ks) {
#pragma unroll
        for (int j = 0; j < 4; ++j) {
          acc[j] = MFMA16(bq[ks % 3][j], as_frag(ha[ks]), acc[j]);
          if (ks + 2 < 14) rdB(ks + 2, j);
          if (ks == 12) xr[j] = *(const u32x2*)(smem + L_X + trow * 128 + (((2 * j + (g >> 1)) ^ (trow & 7)) << 4) + (g & 1) * 8);
          SB();
        }
      }
      STAMP(11);
      float sum = 0.f, sq = 0.f;
      u32x4 x1k[2];
#pragma unroll
      for (int j = 0; j < 4; ++j) {
        unsigned lo = pack2(acc[j][0] + bf_lo(xr[j][0]), acc[j][1] + bf_hi(xr[j][0]));
        unsigned hi = pack2(acc[j][2] + bf_lo(xr[j][1]), acc[j][3] + bf_hi(xr[j][1]));
        x1k[j >> 1][2 * (j & 1)] = lo; x1k[j >> 1][2 * (j & 1) + 1] = hi;
        float e0 = bf_lo(lo), e1 = bf_hi(lo), e2 = bf_lo(hi), e3 = bf_hi(hi);
        sum += (e0 + e1) + (e2 + e3);
        sq += (e0 * e0 + e1 * e1) + (e2 * e2 + e3 * e3);
      }
#pragma unroll
      for (int jj = 0; jj < 2; ++jj) st16_sc1(rs, OFF_X1 + (((rt64 * 4 + w) * 14 + 2 * c7 + jj) * 64 + lane) * 16, x1k[jj]);
      u32x2 t = {__float_as_uint(xsum4(sum)), __float_as_uint(xsum4(sq))};
      if (g == 0) st8_sc1(rs, OFF_ST + (rt64 * 4 + w) * 1024 + (c7 >> 1) * 256 + r16 * 16 + (c7 & 1) * 8, t);
    }
    ev_signal(smem, 8, w, lane);
    WAIT_V(0);
    post_flags(ws, 2 * (rt64 & 1) + (w >> 1), cons3, 14, c7 * 2 + (w & 1), lane);
    STAMP(2);
    ev_wait(smem, 8);
    loadW(w3b, F_ * 2, 7 * w, 7, l0);
    loadW(w3b + 128, F_ * 2, 22 + w, 1, l0 + L_H1);
    STAMP(3);
    wait_flags(ws, w, 14, lane);
    ev_signal(smem, 2, w, lane);
    STAMP(4);
    {
      u32x4 a[2][14];
      const unsigned aoff = OFF_X1 + ((panel * 8 + 2 * w) * 14 * 64 + lane) * 16;
      auto ldA = [&](int ks) {
        a[0][ks] = ld16_sc1(rs, aoff + ks * 1024);
        a[1][ks] = ld16_sc1(rs, aoff + 14336 + ks * 1024);
      };
#pragma unroll
      for (int ks = 0; ks < 6; ++ks) ldA(ks);
      ev_wait(smem, 2);
      ev_wait(smem, 6);
      STAMP(5);
      u32x4 stv[2];
      float g1[2];
      f32x4 c4[8];
      f32x4 acc[2][8] = {};
      bf16x8 bq[8];
      auto rdB = [&](int n) {
        const int ks = n >> 3, j = n & 7;
        const int base = j < 4 ? ks * 4096 : (ks < 13 ? L_H1 + ks * 4096 : L_C);
        const char* bp = smem + base + (btr ^ ((j & 3) << 5));
        return ld_tr2(bp, bp + 2048);
      };
#pragma unroll
      for (int n = 0; n < 6; ++n) bq[n] = rdB(n);
      SB();
#pragma unroll
      for (int ks = 0; ks < 14; ++ks) {
        if (ks == 12) ev_wait(smem, 7);
        if (ks == 6 && lane == 0) *(volatile LDS_AS unsigned*)(LDS_AS char*)(smem + L_EV + 15 * 16 + w * 4) = 1u;
#pragma unroll
        for (int j = 0; j < 8; ++j) {
          const int n = ks * 8 + j;
          acc[0][j] = MFMA16(bq[j], as_frag(a[0][ks]), acc[0][j]);
          acc[1][j] = MFMA16(bq[j], as_frag(a[1][ks]), acc[1][j]);
          if (n + 6 < 112) bq[(j + 6) & 7] = rdB(n + 6);
          if ((j == 1 || j == 5) && ks < 4) ldA(2 * ks + 6 + (j >> 2));
          if (ks == 4 && j < 2) {
            stv[j] = ld16_sc1(rs, OFF_ST + (panel * 8 + 2 * w + j) * 1024 + lane * 16);
            g1[j] = ((const float*)(ws + OFF_G))[(panel * 128 + 32 * w + 16 * j + r16) * 2 + 1];
          }
          if (ks == 5) c4[j] = *(const f32x4*)(ws + OFF_CS + (size_t)(c14 * 128 + 16 * j + 4 * g) * 4);
          SB();
        }
      }
      ev_signal(smem, 3, w, lane);
      STAMP(6);
#pragma unroll
      for (int rf = 0; rf < 2; ++rf) {
        float s1 = __uint_as_float(stv[rf][0]), s2 = __uint_as_float(stv[rf][1]);
        if (g < 3) { s1 += __uint_as_float(stv[rf][2]); s2 += __uint_as_float(stv[rf][3]); }
        s1 = xsum4(s1); s2 = xsum4(s2);
        float mean = s1 * (1.f / D_);
        float var = fmaxf(s2 * (1.f / D_) - mean * mean, 0.f);
        float a_ = __builtin_amdgcn_rsqf(var + 1e-5f);
        float k1 = GC1 * a_, k3 = GC3 * a_ * a_ * a_, inv = __builtin_amdgcn_rcpf(fmaxf(a_ * g1[rf], 1e-30f));
        const f32x2 nm = {-mean, -mean};
#pragma unroll
        for (int jj = 0; jj < 4; ++jj) {
          u32x4 v;
#pragma unroll
          for (int h = 0; h < 2; ++h) {
            const int j = 2 * jj + h;
            f32x2 z0 = __builtin_elementwise_fma(nm, f32x2{c4[j][0], c4[j][1]}, f32x2{acc[rf][j][0], acc[rf][j][1]});
            f32x2 z1 = __builtin_elementwise_fma(nm, f32x2{c4[j][2], c4[j][3]}, f32x2{acc[rf][j][2], acc[rf][j][3]});
            v[2 * h] = pack2v(gelu2(z0, k1, k3, inv));
            v[2 * h + 1] = pack2v(gelu2(z1, k1, k3, inv));
          }
          st16_sc1(rs, OFF_AC + (((panel * 8 + 2 * w + rf) * 72 + 16 + c14 * 4 + jj) * 64 + lane) * 16, v);
        }
      }
    }
    STAMP(7);
    WAIT_V(0);
    STAMP(12);
    post_flags(ws, c14 < 7 ? 6 : 5, ((4 * lane + 2 * (panel & 1) + (w >> 1)) << 3) | xcd, 7, (c14 < 7 ? c14 : c14 - 7) * 2 + (w & 1), lane);
    __builtin_amdgcn_s_setprio(0);
    ev_wait(smem, 14);
#pragma unroll
    for (int m = 0; m < 7; ++m) {
      const int n = 7 * w + m;
      const char* sl = smem + (n < 27 ? n * 4096 : L_C) + lane * 16;
#pragma unroll
      for (int j = 0; j < 4; ++j) bp[m][j] = ld_frag(sl + ((j << 10) ^ ((w8 & 1) << 11)));
    }
    ev_signal(smem, 16, w, lane);
    STAMP(8);

  } else {
    const int gb = (7 * head) >> 1;
    const int qw = qb * 128 + 32 * w;
    BAR();
    __builtin_amdgcn_s_setprio(3);
    {
      const unsigned lv = (lane & 48) | ((lane & 15) ^ ((lane & 16) >> 1));
      const char* src = ws + OFF_QKV + (size_t)(56 + gb) * 256;
#pragma unroll
      for (int m = 0; m < 6; ++m) {
        const int lt = 6 * w + m;
        int kt = qb * 8 - 16 + lt;
        kt = kt < 0 ? 0 : kt;
        glds16(src + (size_t)kt * 43008, lane * 16, l0 + L_C + lt * 1024);
        glds16(src + (size_t)kt * 43008 + 56 * 256, lv * 16, l0 + L_C + 24576 + lt * 1024);
      }
    }
    u32x4 qv[2];
#pragma unroll
    for (int h = 0; h < 2; ++h)
      asm volatile("global_load_dwordx4 %0, %1, %2" : "=v"(qv[h]) : "v"((((qb * 8 + 2 * w + h) * 168 + gb) * 16 + lane) * 16), "s"(ws + OFF_QKV) : "memory");
    if (has_gemm) {
      const unsigned o0 = (88 * w + r8) * (F_ * 2) + ((w & 1) ? cO : cE), o1 = (88 * w + r8) * (F_ * 2) + ((w & 1) ? cE : cO);
      const unsigned ld = l0 + L_H1 + w * 11264;
#pragma unroll
      for (int m = 0; m < 11; ++m) glds16(w3b + 128, ((m & 1) ? o1 : o0) + m * 8 * F_ * 2, ld + m * 1024);
      WAIT_V(11);
    } else {
      WAIT_V(0);
    }
    asm volatile("" : "+v"(qv[0]), "+v"(qv[1])::"memory");
    ev_signal(smem, 4, w, lane);
    ev_wait(smem, 4);
    STAMP(1);
    const char* sK = smem + L_C;
    const char* sV = smem + L_C + 24576;
    const bool odd = head & 1;
    const bool z01 = odd && g == 0, z23 = !odd && g == 3;
    bf16x8 qf[2];
#pragma unroll
    for (int h = 0; h < 2; ++h) {
      qv[h][0] = z01 ? 0u : qv[h][0]; qv[h][1] = z01 ? 0u : qv[h][1];
      qv[h][2] = z23 ? 0u : qv[h][2]; qv[h][3] = z23 ? 0u : qv[h][3];
      qf[h] = as_frag(qv[h]);
    }
    const float NEG = -__builtin_inff();
    constexpr float SC = 0.1889822365046136f * 1.4426950408889634f;
    f32x4 s[2][17];
    float mx[2] = {NEG, NEG};
    auto max4 = [&](float m, const f32x4& v) {
      return __builtin_fmaxf(__builtin_fmaxf(__builtin_fmaxf(__builtin_fmaxf(m, v[0]), v[1]), v[2]), v[3]);
    };
    auto finish = [&](int h, int t) {
      if (t == 0 || t == 16) {
#pragma unroll
        for (int i = 0; i < 4; ++i) {
          const int b = 4 * g + i;
          s[h][t][i] = ((b > r16) == (t == 0)) ? s[h][t][i] : NEG;
        }
      }
      mx[h] = max4(mx[h], s[h][t]);
    };
    {
      bf16x8 kf[6];
#pragma unroll
      for (int t = 0; t < 6; ++t) kf[t] = ld_frag(sK + (2 * w + t) * 1024 + lane * 16);
      SB();
#pragma unroll
      for (int t = 0; t < 20; ++t) {
        if (t < 18) {
          f32x4 z = {};
          if (t < 17) s[0][t] = MFMA16(kf[t % 6], qf[0], z);
          if (t > 0) s[1][t - 1] = MFMA16(kf[t % 6], qf[1], z);
          if (t + 6 < 18) kf[t % 6] = ld_frag(sK + (2 * w + t + 6) * 1024 + lane * 16);
        }
        if (t >= 2) {
          if (t - 2 < 17) finish(0, t - 2);
          if (t - 2 > 0) finish(1, t - 3);
        }
        SB();
      }
    }
    if (qw < 256) {
#pragma unroll
      for (int h = 0; h < 2; ++h) {
        const int tstart = 16 - (qw >> 4) - h;
        mx[h] = NEG;
#pragma unroll
        for (int t = 0; t < 17; ++t) {
          if (t < tstart) s[h][t] = f32x4{NEG, NEG, NEG, NEG};
          mx[h] = max4(mx[h], s[h][t]);
        }
      }
    }
    const float mc[2] = {-xmax4(mx[0]) * SC, -xmax4(mx[1]) * SC};
    STAMP(2);
    WAIT_V(0);
    ev_signal(smem, 6, w, lane);
    float gt[2];
#pragma unroll
    for (int h = 0; h < 2; ++h) gt[h] = ((const float*)(ws + OFF_G))[(qw + 16 * h + r16) * 2];
    STAMP(3);
    f32x4 o[2][2] = {}, lsum[2] = {};
    bf16x8 ones;
#pragma unroll
    for (int i = 0; i < 8; ++i) ones[i] = (bf16)1.0f;
    auto pexp = [&](const f32x4& sv, float m, unsigned& lo, unsigned& hi) {
      f32x2 a = __builtin_elementwise_fma(f32x2{sv[0], sv[1]}, f32x2{SC, SC}, f32x2{m, m});
      f32x2 b = __builtin_elementwise_fma(f32x2{sv[2], sv[3]}, f32x2{SC, SC}, f32x2{m, m});
      lo = pack2(__builtin_amdgcn_exp2f(a[0]), __builtin_amdgcn_exp2f(a[1]));
      hi = pack2(__builtin_amdgcn_exp2f(b[0]), __builtin_amdgcn_exp2f(b[1]));
    };
    const int voff0 = (p >> 1) * 256 + (((4 * g + q) ^ ((p >> 1) << 3)) << 4) + (p & 1) * 8;
    bf16x8 vf[2][2], pf[2][2];
#pragma unroll
    for (int kk = 0; kk < 10; ++kk) {
      if (kk < 9) {
        const char* vb = sV + (2 * w + 2 * kk) * 1024 + voff0;
        vf[kk & 1][0] = ld_tr2(vb, vb + 1024);
        vf[kk & 1][1] = ld_tr2(vb + 512, vb + 1536);
#pragma unroll
        for (int h = 0; h < 2; ++h) {
          unsigned p0 = 0, p1 = 0, p2 = 0, p3 = 0;
          const int t0 = 2 * kk - h, t1 = t0 + 1;
          if (t0 >= 0) pexp(s[h][t0 < 0 ? 0 : t0], mc[h], p0, p1);
          if (t1 <= 16) pexp(s[h][t1 > 16 ? 16 : t1], mc[h], p2, p3);
          pf[kk & 1][h] = as_frag(u32x4{p0, p1, p2, p3});
        }
      }
      if (kk > 0) {
        const int c = (kk - 1) & 1;
#pragma unroll
        for (int h = 0; h < 2; ++h) {
          o[h][0] = MFMA16(vf[c][0], pf[c][h], o[h][0]);
          o[h][1] = MFMA16(vf[c][1], pf[c][h], o[h][1]);
          lsum[h] = MFMA16(ones, pf[c][h], lsum[h]);
        }
      }
      SGB(0x100, 4);
#pragma unroll
      for (int m = 0; m < 6; ++m) { SGB(0x8, 1); SGB(0x402, 5); }
      SB();
    }
    __builtin_amdgcn_s_setprio(3);
    ev_signal(smem, 5, w, lane);
    STAMP(4);
#pragma unroll
    for (int h = 0; h < 2; ++h) {
      float sc = gt[h] * __builtin_amdgcn_rcpf(lsum[h][0]);
      u32x4 v;
      v[0] = z01 ? 0u : pack2(o[h][0][0] * sc, o[h][0][1] * sc);
      v[1] = z01 ? 0u : pack2(o[h][0][2] * sc, o[h][0][3] * sc);
      v[2] = z23 ? 0u : pack2(o[h][1][0] * sc, o[h][1][1] * sc);
      v[3] = z23 ? 0u : pack2(o[h][1][2] * sc, o[h][1][3] * sc);
      st16_sc1(rs, OFF_AC + (((qb * 8 + 2 * w + h) * 72 + head) * 64 + lane) * 16, v);
    }
    if (!has_gemm) {
      WAIT_V(0);
      ev_signal(smem, 11, w, lane);
      if (w == 0) {
        ev_wait(smem, 11);
        post_flags(ws, 4, cons4, 14, head, lane);
      }
      return;
    }
    ev_wait(smem, 5);
    glds16(w3b + 128 + (size_t)(416 + 8 * w) * F_ * 2, r8 * (F_ * 2) + ((w & 1) ? cO : cE), l0 + L_C + w * 1024);
  }
  {
    unsigned aoff[4];
#pragma unroll
    for (int rt = 0; rt < 4; ++rt) aoff[rt] = OFF_AC + (((rt64 * 4 + (rt ^ (w8 >> 1))) * 72 + w) * 64 + lane) * 16;
    f32x4 acc[4][4] = {};
    u32x4 a[3][4];
    auto ldA = [&](int buf, int k4) {
#pragma unroll
      for (int rt = 0; rt < 4; ++rt) a[buf][rt] = ld16_sc1(rs, aoff[rt] + k4 * 4096);
    };
    auto mma = [&](const u32x4 (&af)[4], const bf16x8 (&bf)[4]) {
#pragma unroll
      for (int rt = 0; rt < 4; ++rt)
#pragma unroll
        for (int j = 0; j < 4; ++j) acc[rt][j] = MFMA16(bf[j], as_frag(af[rt]), acc[rt][j]);
    };
    if (grp == 1) {
      bf16x8 bo[4];
      WAIT_V(0);
      ev_signal(smem, 7, w, lane);
      STAMP(5);
      if (w == 0) {
        ev_wait(smem, 7);
        post_flags(ws, 4, cons4, 14, head, lane);
      }
      ev_wait(smem, 15);
      ldB(bp[6], 0); ldB(bo, 1);
#pragma unroll
      for (int m = 0; m < 6; ++m) ldB(bp[m], 4 + m);
      STAMP(6);
#pragma unroll
      for (int m = 0; m < 7; ++m)
#pragma unroll
        for (int j = 0; j < 4; ++j) asm volatile("" : "+v"(bp[m][j]));
#pragma unroll
      for (int j = 0; j < 4; ++j) asm volatile("" : "+v"(bo[j]));
      ev_wait(smem, 3);
#pragma unroll
      for (int m = 0; m < 7; ++m) {
        const int n = 7 * w + m;
        const unsigned dst = l0 + (n < 27 ? n * 4096 : L_C);
        const char* src = ws + OFF_BT + (size_t)(c7 * 72 + 44 + w + 4 * m) * 4096;
        glds16<0>(src, lane * 16, dst); glds16<1024>(src, lane * 16, dst);
        glds16<2048>(src, lane * 16, dst); glds16<3072>(src, lane * 16, dst);
        WAIT_V(8);
      }
      if (w == 0) {
        wait_flags(ws, 4, 16, lane);
        ev_signal_all(smem, 12, lane);
      } else {
        ev_wait(smem, 12);
      }
      ldA(0, 0); ldA(1, 1);
      WAIT_V(8);
      ev_signal(smem, 14, w, lane);
      STAMP(7);
      __builtin_amdgcn_s_setprio(0);
      SB();
      mma(a[0], bp[6]);
      SB();
      ldB(bp[6], 2); ldA(0, 2);
      SB();
      mma(a[1], bo);
      SB();
      ldB(bo, 3); ldA(1, 3);
      SB();
      mma(a[0], bp[6]);
      SB();
      ldB(bp[6], 10);
      SB();
      mma(a[1], bo);
      SB();
      STAMP(8);
    }
    if (w == 0) {
      wait_flags(ws, 5 + grp, 14, lane);
      ev_signal_all(smem, 18 + grp, lane);
    } else {
      ev_wait(smem, 18 + grp);
    }
    STAMP(9);
    const int k0 = grp ? 4 : 11;
#pragma unroll
    for (int m = 0; m < 3; ++m) ldA(m, k0 + m);
    const int tt = rt64 * 4 + (w8 >> 1);
    u32x4 x1v;
    float g1o;
    SB();
#pragma unroll
    for (int m = 0; m < 7; ++m) {
      mma(a[m % 3], bp[m]);
      SB();
      if (m + 3 < 7) ldA(m % 3, k0 + m + 3);
      if (m == 4) {
        x1v = ld16_sc1(rs, OFF_X1 + ((tt * 14 + 2 * c7 + (w8 & 1)) * 64 + lane) * 16);
        g1o = ((const float*)(ws + OFF_G))[(tt * 16 + r16) * 2 + 1];
      }
      SB();
    }
    STAMP(10);
    ev_wait(smem, 16);
    auto exa = [&](int dst) { return (f32x4*)(smem + dst * 14336 + (dst == 7 ? L_C - 7 * 14336 : 0)) + lane; };
#pragma unroll
    for (int pos = 1; pos < 8; ++pos)
#pragma unroll
      for (int h = 0; h < 2; ++h) exa(w8 ^ pos)[((pos - 1) * 2 + h) * 64] = acc[pos >> 1][2 * (pos & 1) + h];
    BAR();
    STAMP(13);
    unsigned pk[2][2];
#pragma unroll
    for (int h = 0; h < 2; ++h) {
      f32x4 r = acc[0][h];
#pragma unroll
      for (int sidx = 0; sidx < 7; ++sidx) r += exa(w8)[(sidx * 2 + h) * 64];
      pk[h][0] = pack2(__builtin_fmaf(g1o, bf_lo(x1v[2 * h]), r[0]), __builtin_fmaf(g1o, bf_hi(x1v[2 * h]), r[1]));
      pk[h][1] = pack2(__builtin_fmaf(g1o, bf_lo(x1v[2 * h + 1]), r[2]), __builtin_fmaf(g1o, bf_hi(x1v[2 * h + 1]), r[3]));
    }
    auto s0 = __builtin_amdgcn_permlane16_swap(pk[0][0], pk[1][0], false, false);
    auto s1 = __builtin_amdgcn_permlane16_swap(pk[0][1], pk[1][1], false, false);
    u32x4 v = {s0[0], s1[0], s0[1], s1[1]};
    const int gl = (g >> 1) | ((g & 1) << 1);
    *(u32x4*)(out + (size_t)(tt * 16 + r16) * D_ + c7 * 64 + 32 * (w8 & 1) + 8 * gl) = v;
  }
  STAMP_FLUSH(ws, 1);
}

extern "C" void kernel_launch(void* const* d_in, const int* in_sizes, int n_in, void* d_out, int out_size, void* d_ws,
                              size_t ws_size, hipStream_t stream) {
  const bf16* x = (const bf16*)d_in[0];
  const bf16* wqkv = (const bf16*)d_in[1];
  const bf16* wo = (const bf16*)d_in[2];
  const bf16* w1 = (const bf16*)d_in[4];
  const bf16* w2 = (const bf16*)d_in[5];
  const bf16* w3 = (const bf16*)d_in[6];
  const bf16* w4 = (const bf16*)d_in[7];
  const bf16* gw = (const bf16*)d_in[8];
  char* ws = (char*)d_ws;
  k1_qkv_h<<<256, 384, 0, stream>>>(x, wqkv, w1, w2, w3, w4, wo, gw, ws);
  k2_mega<<<256, 512, 0, stream>>>(x, w2, w3, w4, wo, ws, (bf16*)d_out);
}
```

```cpp
#include <hip/hip_runtime.h>
#include <hip/hip_bf16.h>

typedef __bf16 bf16;
typedef __bf16 bf16x2 __attribute__((ext_vector_type(2)));
typedef __bf16 bf16x4 __attribute__((ext_vector_type(4)));
typedef __bf16 bf16x8 __attribute__((ext_vector_type(8)));
typedef float f32x2 __attribute__((ext_vector_type(2)));
typedef float f32x4 __attribute__((ext_vector_type(4)));
typedef unsigned u32x2 __attribute__((ext_vector_type(2)));
typedef unsigned u32x4 __attribute__((ext_vector_type(4)));
#define LDS_AS __attribute__((address_space(3)))
#define DI __device__ __forceinline__

constexpr int S_ = 2048, D_ = 448, F_ = 1792;
constexpr size_t OFF_QKV = 0;
constexpr size_t OFF_HS = OFF_QKV + 5505024;
constexpr size_t OFF_X1 = OFF_HS + 1835008;
constexpr size_t OFF_AC = OFF_X1 + 1835008;
constexpr size_t OFF_ST = OFF_AC + 9437184;
constexpr size_t OFF_G  = OFF_ST + 262144;
constexpr size_t OFF_CS = OFF_G + 16384;
constexpr size_t OFF_FL = OFF_CS + 8192;
constexpr size_t OFF_BT = OFF_FL + 256 * 8 * 128;
constexpr size_t WS_USED = OFF_BT + 7 * 72 * 4096;

#define STAMP_DECL
#define STAMP(i)
#define STAMP_FLUSH(ws, kid)
#define STAMP_ARG
#define STAMP_PASS

#define WAIT_V(n) asm volatile("s_waitcnt vmcnt(" #n ")" ::: "memory")
#define BAR() do { asm volatile("s_waitcnt lgkmcnt(0)" ::: "memory"); __builtin_amdgcn_s_barrier(); asm volatile("" ::: "memory"); } while (0)
#define MFMA16(a, b, c) __builtin_amdgcn_mfma_f32_16x16x32_bf16((a), (b), (c), 0, 0, 0)
#define MFMA16_IP(a, b, c) asm volatile("v_mfma_f32_16x16x32_bf16 %0, %1, %2, %0" : "+v"(c) : "v"(a), "v"(b))
#define MFMA_DRAIN() asm volatile("s_nop 7\n\ts_nop 1")

template <int IMM = 0>
DI void glds16(const void* sbase, unsigned voff, unsigned lds) {
  asm volatile("s_mov_b32 m0, %2\n\ts_nop 0\n\tglobal_load_lds_dwordx4 %0, %1 offset:%3" ::"v"(voff), "s"(sbase), "s"(lds), "i"(IMM) : "memory");
}
template <int LOFF>
DI void glds16c(const void* sbase, unsigned voff, unsigned ldsbase) {
  asm volatile("s_add_u32 m0, %2, %3\n\ts_nop 0\n\tglobal_load_lds_dwordx4 %0, %1" ::"v"(voff), "s"(sbase), "s"(ldsbase), "i"(LOFF) : "memory", "scc");
}
DI void touch8k(unsigned& sink, const void* sbase, unsigned chunk, int lane) {
  asm volatile("global_load_dword %0, %1, %2" : "+v"(sink) : "v"(chunk * 8192u + lane * 128u), "s"(sbase) : "memory");
}
DI void touch_keep(unsigned& sink) { asm volatile("" : "+v"(sink)::"memory"); }
DI unsigned lds_addr(const char* p) { return (unsigned)(size_t)(const LDS_AS char*)p; }
DI bf16x8 ld_frag(const char* lds) { return *(const bf16x8*)lds; }
DI bf16x8 ld_tr2(const char* p0, const char* p1) {
  bf16x4 a = __builtin_amdgcn_ds_read_tr16_b64_v4bf16((LDS_AS bf16x4*)p0);
  bf16x4 b = __builtin_amdgcn_ds_read_tr16_b64_v4bf16((LDS_AS bf16x4*)p1);
  return __builtin_shufflevector(a, b, 0, 1, 2, 3, 4, 5, 6, 7);
}
DI bf16x8 ld_tr2a(unsigned a0, unsigned a1) {
  bf16x4 a = __builtin_amdgcn_ds_read_tr16_b64_v4bf16((LDS_AS bf16x4*)(size_t)a0);
  bf16x4 b = __builtin_amdgcn_ds_read_tr16_b64_v4bf16((LDS_AS bf16x4*)(size_t)a1);
  return __builtin_shufflevector(a, b, 0, 1, 2, 3, 4, 5, 6, 7);
}
DI bf16x8 ld_fraga(unsigned a) { return *(const LDS_AS bf16x8*)(size_t)a; }
DI unsigned opaque(unsigned v) { asm volatile("" : "+v"(v)); return v; }
DI unsigned pack2(float a, float b) {
  f32x2 t = {a, b};
  return __builtin_bit_cast(unsigned, __builtin_convertvector(t, bf16x2));
}
DI float bf_lo(unsigned u) { return __builtin_bit_cast(float, u << 16); }
DI float bf_hi(unsigned u) { return __builtin_bit_cast(float, u & 0xffff0000u); }
DI float xsum4(float x) {
  auto a = __builtin_amdgcn_permlane16_swap(__float_as_uint(x), __float_as_uint(x), false, false);
  x = __uint_as_float(a[0]) + __uint_as_float(a[1]);
  auto b = __builtin_amdgcn_permlane32_swap(__float_as_uint(x), __float_as_uint(x), false, false);
  return __uint_as_float(b[0]) + __uint_as_float(b[1]);
}
DI float xmax4(float x) {
  auto a = __builtin_amdgcn_permlane16_swap(__float_as_uint(x), __float_as_uint(x), false, false);
  x = fmaxf(__uint_as_float(a[0]), __uint_as_float(a[1]));
  auto b = __builtin_amdgcn_permlane32_swap(__float_as_uint(x), __float_as_uint(x), false, false);
  return fmaxf(__uint_as_float(b[0]), __uint_as_float(b[1]));
}

DI float gelu_ln(float z, float k1, float k3, float inv) {
  float t = z * z;
  float w = __builtin_fmaf(t, k3, k1);
  float e = __builtin_amdgcn_exp2f(z * w);
  float d = __builtin_fmaf(e, inv, inv);
  return z * __builtin_amdgcn_rcpf(d);
}
constexpr float GC1 = -2.0f * 0.7978845608028654f * 1.4426950408889634f;
constexpr float GC3 = GC1 * 0.044715f;

DI int swz(int row) { return ((row >> 1) & 1) | ((((row >> 2) ^ (row >> 3)) & 1) << 1); }
DI int src_chunk(int row, int lane) { return ((((lane & 7) >> 1) ^ swz(row)) << 1) | (lane & 1); }

template <int N> DI void wait_v() { asm volatile("s_waitcnt vmcnt(%0)" ::"i"(N) : "memory"); }
#define SB() __builtin_amdgcn_sched_barrier(0)
#define SGB(mask, n) __builtin_amdgcn_sched_group_barrier(mask, n, 0)
DI f32x2 gelu2(f32x2 z, float k1, float k3, float inv) {
  f32x2 t = z * z;
  f32x2 w = __builtin_elementwise_fma(t, f32x2{k3, k3}, f32x2{k1, k1});
  f32x2 u = z * w;
  f32x2 e = {__builtin_amdgcn_exp2f(u[0]), __builtin_amdgcn_exp2f(u[1])};
  f32x2 d = __builtin_elementwise_fma(e, f32x2{inv, inv}, f32x2{inv, inv});
  f32x2 r = {__builtin_amdgcn_rcpf(d[0]), __builtin_amdgcn_rcpf(d[1])};
  return z * r;
}
DI unsigned pack2v(f32x2 t) { return __builtin_bit_cast(unsigned, __builtin_convertvector(t, bf16x2)); }

#define CODE_ADDR(p, sym) do { int off_; asm volatile("s_getpc_b64 %0\n\ts_mov_b32 %1, " #sym "@rel32@lo+4" : "=s"(p), "=s"(off_)); p += off_; } while (0)
constexpr unsigned K2_CODE_BYTES = 26000;

#define NTOUCH 5
constexpr int K1_A = 114688;
template <bool STATS>
DI void k1_compute(char* smem, const char* xrows, int w, int lane, f32x4 (&acc)[2][8], f32x4& cs, f32x4 (&sum)[2],
                   f32x4 (&sq)[2] STAMP_ARG) {
  const int g = lane >> 4, q = (lane >> 2) & 3, p = lane & 3, r16 = lane & 15;
  const unsigned la = lds_addr(smem) + K1_A + w * 12288;
  const unsigned xoff = ((32 * w + (lane >> 3)) * D_ + (((lane & 7) ^ (lane >> 3)) << 3)) * 2;
  auto issueA = [&](int st, int slot) {
#pragma unroll
    for (int i = 0; i < 4; ++i) glds16(xrows + st * 128, xoff + i * 8 * D_ * 2, la + slot * 4096 + i * 1024);
  };
  issueA(0, 0); issueA(1, 1); issueA(2, 2);
  const int a0 = r16 * 128 + ((g ^ (r16 & 7)) << 4);
  const unsigned ab[2] = {opaque(la + a0), opaque(la + (a0 ^ 64))};
  const int b0 = (8 * g + q) * 128 + (swz(8 * g + q) << 5) + p * 8;
  unsigned hb[2][4];
#pragma unroll
  for (int h = 0; h < 2; ++h)
#pragma unroll
    for (int m = 0; m < 4; ++m) hb[h][m] = opaque(lds_addr(smem) + h * 57344 + (b0 ^ (m << 5)));
  const unsigned wb0 = opaque(lds_addr(smem) + 57344 + (b0 ^ (w << 5))), wb1 = opaque(lds_addr(smem) + 57344 + (b0 ^ ((w ^ 2) << 5)) + 512);
  bf16x8 ones;
#pragma unroll
  for (int i = 0; i < 8; ++i) ones[i] = (bf16)1.0f;
  bf16x8 A[2][2], B[2][8], Bw[2];
  auto rdA = [&](int aoff, int boff, int u) {
    A[u][0] = ld_fraga(ab[u] + aoff);
    A[u][1] = ld_fraga(ab[u] + aoff + 2048);
    if (STATS) Bw[u] = ld_tr2a(wb0 + boff, wb1 + boff);
  };
  auto rdB = [&](int boff, int u, int j) { B[u][j] = ld_tr2a(hb[j >> 2][j & 3] + boff, hb[j >> 2][(j & 3) ^ 2] + boff + 512); };
  auto kstep = [&](int u, int nabase, int nbbase, bool more) {
#pragma unroll
    for (int j = 0; j < 8; ++j) {
      acc[0][j] = MFMA16(B[u][j], A[u][0], acc[0][j]);
      acc[1][j] = MFMA16(B[u][j], A[u][1], acc[1][j]);
      if (more) { if (j == 0) rdA(nabase, nbbase, u ^ 1); rdB(nbbase, u ^ 1, j); }
      if (STATS) {
        if (j == 1) sum[0] = MFMA16(ones, A[u][0], sum[0]);
        if (j == 2) sum[1] = MFMA16(ones, A[u][1], sum[1]);
        if (j == 4) sq[0] = MFMA16(A[u][0], A[u][0], sq[0]);
        if (j == 5) sq[1] = MFMA16(A[u][1], A[u][1], sq[1]);
        if (j == 7) cs = MFMA16(Bw[u], ones, cs);
      }
      SB();
    }
  };
#pragma unroll
  for (int j = 0; j < 8; ++j) asm volatile("" : "+v"(acc[0][j]), "+v"(acc[1][j]));
  if (STATS) asm volatile("" : "+v"(sum[0]), "+v"(sum[1]), "+v"(sq[0]), "+v"(sq[1]), "+v"(cs));
  STAMP(1);
  WAIT_V(8);
  BAR(); STAMP(2);
  rdA(0, 0, 0);
#pragma unroll
  for (int j = 0; j < 8; ++j) rdB(0, 0, j);
  SB();
#pragma unroll
  for (int s = 0; s < 6; ++s) {
    kstep(0, (s % 3) * 4096, s * 8192 + 4096, true);
    if (s < 5) WAIT_V(4); else WAIT_V(0);
    if (s < 4) asm volatile("s_waitcnt lgkmcnt(14)" ::: "memory");
    if (s < 2 || s == 3) __builtin_amdgcn_s_barrier();
    asm volatile("" ::: "memory");
    if (s < 4) issueA(s + 3, s % 3);
    kstep(1, ((s + 1) % 3) * 4096, s * 8192 + 8192, true);
  }
  kstep(0, 0, 6 * 8192 + 4096, true);
  kstep(1, 0, 0, false);
  STAMP(9);
}

extern "C" __global__ __launch_bounds__(384, 2) void k1_qkv_h(const bf16* __restrict__ x, const bf16* __restrict__ wqkv,
                                                   const bf16* __restrict__ w1, const bf16* __restrict__ w2,
                                                   const bf16* __restrict__ w3, const bf16* __restrict__ w4,
                                                   const bf16* __restrict__ wo, const bf16* __restrict__ gw,
                                                   char* __restrict__ ws) {
  __shared__ __attribute__((aligned(16))) char smem[163840];
  const int tid = threadIdx.x, lane = tid & 63;
  const int w = __builtin_amdgcn_readfirstlane(tid >> 6);
  const int xcd = blockIdx.x & 7, idx = blockIdx.x >> 3;
  STAMP_DECL STAMP(0);
  if (idx >= 28) {
    const int hb = xcd * 4 + (idx - 28);
    float* red = (float*)smem;
    if (tid < 256) {
      ((u32x4*)(ws + OFF_FL))[hb * 512 + tid] = u32x4{0u, 0u, 0u, 0u};
      ((u32x4*)(ws + OFF_FL))[hb * 512 + 256 + tid] = u32x4{0u, 0u, 0u, 0u};
    }
    if (w < 4) {
      const int r8 = lane >> 3;
      const unsigned cE = src_chunk(r8, lane) * 16, cO = src_chunk(r8 + 8, lane) * 16;
      const unsigned lt = lds_addr(smem) + 4096 + w * 16384;
#pragma unroll
      for (int m = 0; m < 4; ++m) {
        int u = hb * 16 + w + 4 * m;
        u = u < 504 ? u : 503;
        const int c7 = u / 72, k = u - c7 * 72;
        const char* src = k < 16 ? (const char*)(wo + (size_t)k * 28 * D_ + c7 * 64) : (const char*)(w4 + (size_t)(k - 16) * 32 * D_ + c7 * 64);
#pragma unroll
        for (int i = 0; i < 4; ++i) {
          int d = 8 * i + r8;
          if (k < 16) { d -= 4 * (k & 1); d = d < 0 ? 0 : (d > 27 ? 27 : d); }
          glds16(src, d * (D_ * 2) + ((i & 1) ? cO : cE), lt + m * 4096 + i * 1024);
        }
      }
      if (hb < 28) {
        float a[8] = {};
#pragma unroll
        for (int i = 0; i < 14; ++i) {
          bf16x8 v = *(const bf16x8*)(w3 + (size_t)(w * 112 + i * 8 + (lane >> 3)) * F_ + hb * 64 + (lane & 7) * 8);
#pragma unroll
          for (int e = 0; e < 8; ++e) a[e] += (float)v[e];
        }
#pragma unroll
        for (int e = 0; e < 8; ++e) {
          a[e] += __shfl_xor(a[e], 8);
          a[e] = xsum4(a[e]);
        }
        if (lane < 8) {
#pragma unroll
          for (int e = 0; e < 8; ++e) red[w * 64 + lane * 8 + e] = a[e];
        }
      }
      WAIT_V(0);
      const int g = lane >> 4, q = (lane >> 2) & 3;
      const int btr = (4 * g + q) * 128 + (swz(4 * g + q) << 5) + (lane & 3) * 8;
#pragma unroll
      for (int m = 0; m < 4; ++m) {
        const int u = hb * 16 + w + 4 * m;
        if (u < 504) {
#pragma unroll
          for (int j = 0; j < 4; ++j) {
            const char* bp = smem + 4096 + w * 16384 + m * 4096 + (btr ^ (j << 5));
            *(bf16x8*)(ws + OFF_BT + (size_t)u * 4096 + j * 1024 + lane * 16) = ld_tr2(bp, bp + 2048);
          }
        }
      }
    } else {
      bf16x8 gv = {};
      if (lane < 56) gv = *(const bf16x8*)(gw + lane * 8);
      float* G = (float*)(ws + OFF_G);
      {
        bf16x8 xv[32];
        const int row0 = hb * 64 + (w - 4) * 32;
#pragma unroll
        for (int r = 0; r < 32; ++r) {
          xv[r] = *(const bf16x8*)(x + (row0 + r) * D_ + (lane < 56 ? lane : 0) * 8);
        }
        float mine = 0.f;
#pragma unroll
        for (int r = 0; r < 32; ++r) {
          float s = 0.f;
#pragma unroll
          for (int i = 0; i < 4; ++i) {
            bf16x2 a = {xv[r][2 * i], xv[r][2 * i + 1]}, b = {gv[2 * i], gv[2 * i + 1]};
            s = __builtin_amdgcn_fdot2_f32_bf16(a, b, s, false);
          }
#pragma unroll
          for (int o = 8; o; o >>= 1) s += __shfl_xor(s, o);
          s = xsum4(s);
          mine = lane == r ? s : mine;
        }
        if (lane < 32) {
          f32x2 gg = {1.f / (1.f + __expf(-mine)), 1.f / (1.f + __expf(mine))};
          *(f32x2*)(G + (row0 + lane) * 2) = gg;
        }
      }
      {
        unsigned sink = 0;
        const char* code;
        CODE_ADDR(code, k2_mega);
        const unsigned off = min((unsigned)((((idx & 1) * 2 + w - 4) * 64 + lane) * 128), K2_CODE_BYTES);
        asm volatile("global_load_dword %0, %1, %2" : "+v"(sink) : "v"(off), "s"(code) : "memory");
        WAIT_V(0); touch_keep(sink);
      }
    }
    BAR();
    if (hb < 28 && tid < 64) ((float*)(ws + OFF_CS))[hb * 64 + tid] = (red[tid] + red[64 + tid]) + (red[128 + tid] + red[192 + tid]);
    STAMP_FLUSH(ws, 0);
    return;
  }
  const int panel = 4 * (xcd >> 1) + (idx & 3), ct = 7 * (xcd & 1) + (idx >> 2);
  const bool mixed = ct < 7;
  const int u0 = mixed ? ct : 2 * ct - 7;
  if (w >= 4) {
    const int r = lane >> 3;
    const bool second = w == 5;
    const unsigned l0 = lds_addr(smem) + (second ? 57344 : 0);
    const char* base = (second && mixed) ? (const char*)(w1 + ct * 64) : (const char*)(wqkv + (u0 + (second ? 1 : 0)) * 64);
    const int ld = (second && mixed) ? 896 : 2688;
    const unsigned cE = src_chunk(r, lane) * 16, cO = src_chunk(r + 8, lane) * 16;
    auto issue = [&](int st) {
#pragma unroll
      for (int i = 0; i < 8; ++i) glds16(base + (size_t)st * 64 * ld, (8 * i + r) * ld + ((i & 1) ? cO : cE), l0 + st * 8192 + i * 1024);
    };
    unsigned sink = 0;
#pragma unroll 1
    for (int st = 0; st < 7; ++st) {
      if (st == 2) {
        const char* code;
        CODE_ADDR(code, k1_qkv_h);
        const int c = xcd * 28 + idx;
        const char* pb = second ? code : c < 105 ? (const char*)(wqkv + 128 * 1344) : (const char*)(w1 + 128 * D_);
        touch8k(sink, pb, second ? (idx % 3) : c < 105 ? c : c < 140 ? c - 105 : 0, lane);
        touch8k(sink, (const char*)(x + (size_t)panel * 128 * D_), (idx >> 2) * 2 + (w - 4), lane);
        WAIT_V(10); BAR();
      }
      if (st == 5) { WAIT_V(26); BAR(); }
      issue(st);
    }
    WAIT_V(24); BAR();
    WAIT_V(0); BAR();
    __builtin_amdgcn_s_sleep(4);
#pragma unroll
    for (int m = 0; m < NTOUCH; ++m) {
      const int c = idx * 2 + (w - 4) + 56 * m;
      touch8k(sink, c < 49 ? (const char*)w2 : (const char*)w3, c < 49 ? c : c < 245 ? c - 49 : 0, lane);
    }
    if (mixed) BAR();
    WAIT_V(0);
    touch_keep(sink);
    return;
  }
  const int g = lane >> 4, r16 = lane & 15;
  f32x4 acc[2][8] = {};
  f32x4 cs = {};
  f32x4 sum[2] = {}, sq[2] = {};
  float* ex = (float*)smem;
  const char* xrows = (const char*)(x + (size_t)panel * 128 * D_);
  if (mixed) {
    k1_compute<true>(smem, xrows, w, lane, acc, cs, sum, sq STAMP_PASS);
    if (r16 == 0) *(f32x4*)(ex + w * 16 + 4 * g) = cs;
  } else {
    k1_compute<false>(smem, xrows, w, lane, acc, cs, sum, sq STAMP_PASS);
  }
  {
    const int gl = (g >> 1) | ((g & 1) << 1);
#pragma unroll
    for (int jj = 0; jj < 4; ++jj) {
      if (jj < 2 || !mixed) {
#pragma unroll
        for (int rf = 0; rf < 2; ++rf) {
          const int tt = panel * 8 + 2 * w + rf;
          unsigned a0 = pack2(acc[rf][2 * jj][0], acc[rf][2 * jj][1]), a1 = pack2(acc[rf][2 * jj][2], acc[rf][2 * jj][3]);
          unsigned b0 = pack2(acc[rf][2 * jj + 1][0], acc[rf][2 * jj + 1][1]), b1 = pack2(acc[rf][2 * jj + 1][2], acc[rf][2 * jj + 1][3]);
          auto s0 = __builtin_amdgcn_permlane16_swap(a0, b0, false, false);
          auto s1 = __builtin_amdgcn_permlane16_swap(a1, b1, false, false);
          u32x4 v = {s0[0], s1[0], s0[1], s1[1]};
          *(u32x4*)(ws + OFF_QKV + ((size_t)((tt * 168 + u0 * 8 + 4 * jj + gl) * 16 + r16) << 4)) = v;
        }
      }
    }
  }
  if (mixed) {
    BAR();
    const int nb = ct * 64;
    f32x4 c4[4];
#pragma unroll
    for (int j = 0; j < 4; ++j) c4[j] = *(const f32x4*)(ex + j * 16 + 4 * g);
    unsigned hv[2][4][2];
#pragma unroll
    for (int rf = 0; rf < 2; ++rf) {
      float mean = sum[rf][0] * (1.f / D_);
      float dg = (r16 & 2) ? ((r16 & 1) ? sq[rf][3] : sq[rf][2]) : ((r16 & 1) ? sq[rf][1] : sq[rf][0]);
      dg = xsum4(g == (r16 >> 2) ? dg : 0.f);
      float var = fmaxf(dg * (1.f / D_) - mean * mean, 0.f);
      float a = __builtin_amdgcn_rsqf(var + 1e-5f);
      float k1 = GC1 * a, k3 = GC3 * a * a * a, inv = __builtin_amdgcn_rcpf(a);
      const f32x2 nm = {-mean, -mean};
#pragma unroll
      for (int j = 0; j < 4; ++j) {
        f32x2 z0 = __builtin_elementwise_fma(nm, f32x2{c4[j][0], c4[j][1]}, f32x2{acc[rf][4 + j][0], acc[rf][4 + j][1]});
        f32x2 z1 = __builtin_elementwise_fma(nm, f32x2{c4[j][2], c4[j][3]}, f32x2{acc[rf][4 + j][2], acc[rf][4 + j][3]});
        hv[rf][j][0] = pack2v(gelu2(z0, k1, k3, inv));
        hv[rf][j][1] = pack2v(gelu2(z1, k1, k3, inv));
      }
    }
#pragma unroll
    for (int rf = 0; rf < 2; ++rf)
#pragma unroll
      for (int j = 0; j < 4; ++j) asm volatile("" : "+v"(hv[rf][j][0]), "+v"(hv[rf][j][1]));
#pragma unroll
    for (int jj = 0; jj < 2; ++jj) {
      const int ks = (nb >> 5) + jj;
      int sh[2];
#pragma unroll
      for (int h = 0; h < 2; ++h) {
        const int grp = (nb + 32 * jj + 16 * h) / 112;
        sh[h] = (grp & 2 ? 2 : 1) * (grp & 1 ? -1 : 1);
      }
#pragma unroll
      for (int rf = 0; rf < 2; ++rf) {
        const int tok = panel * 128 + 32 * w + 16 * rf + r16;
        int t2[2];
        u32x2 v[2];
#pragma unroll
        for (int h = 0; h < 2; ++h) {
          t2[h] = tok + sh[h];
          const bool ok = (unsigned)t2[h] < (unsigned)S_;
          t2[h] &= S_ - 1;
          v[h][0] = ok ? hv[rf][2 * jj + h][0] : 0u;
          v[h][1] = ok ? hv[rf][2 * jj + h][1] : 0u;
        }
        char* d0 = ws + OFF_HS + ((size_t)(((t2[0] >> 4) * 14 + ks) * 64 + g * 16 + (t2[0] & 15)) << 4);
        if (ks != 3 && ks != 10) {
          u32x4 vv = {v[0][0], v[0][1], v[1][0], v[1][1]};
          *(u32x4*)d0 = vv;
        } else {
          char* d1 = ws + OFF_HS + ((size_t)(((t2[1] >> 4) * 14 + ks) * 64 + g * 16 + (t2[1] & 15)) << 4) + 8;
          *(u32x2*)d0 = v[0];
          *(u32x2*)d1 = v[1];
        }
      }
    }
  }
  STAMP(10);
  STAMP_FLUSH(ws, 0);
}

typedef __attribute__((address_space(1))) unsigned gu32;
typedef __amdgpu_buffer_rsrc_t rsrc_t;
DI u32x4 ld16_sc1(rsrc_t r, unsigned off) { return __builtin_amdgcn_raw_buffer_load_b128(r, off, 0, 16); }
DI u32x4 ld16(rsrc_t r, unsigned off) { return __builtin_amdgcn_raw_buffer_load_b128(r, off, 0, 0); }
DI void st16_sc1(rsrc_t r, unsigned off, u32x4 v) { __builtin_amdgcn_raw_buffer_store_b128(v, r, off, 0, 16); }
DI void st8_sc1(rsrc_t r, unsigned off, u32x2 v) { __builtin_amdgcn_raw_buffer_store_b64(v, r, off, 0, 16); }
DI bf16x8 as_frag(u32x4 v) { return __builtin_bit_cast(bf16x8, v); }
DI void post_flags(char* ws, int box, int consumer, int n, int word, int lane) {
  if (lane < n)
    __hip_atomic_store((gu32*)(ws + OFF_FL) + (consumer * 8 + box) * 32 + word, 1u, __ATOMIC_RELAXED, __HIP_MEMORY_SCOPE_AGENT);
}
DI void wait_flags(char* ws, int box, int nwords, int lane) {
  gu32* f = (gu32*)(ws + OFF_FL) + (blockIdx.x * 8 + box) * 32 + (lane < nwords ? lane : 0);
#pragma unroll 1
  for (int spin = 0; spin < (1 << 18); ++spin) {
    unsigned v = __hip_atomic_load(f, __ATOMIC_RELAXED, __HIP_MEMORY_SCOPE_AGENT);
    if (__all(v == 1u)) break;
    __builtin_amdgcn_s_sleep(2);
  }
  asm volatile("" ::: "memory");
}
constexpr int L_H1 = 57344, L_X = 106496, L_C = 114688;
enum { EV_W2, EV_W3A, EV_P3, EV_KV, EV_W3B, EV_P2, EV_O, EV_OF, EV_BA, EV_X1, EV_BAR, EV_ATT, EV_H3   };
DI unsigned ev_addr(char* smem, int ev) { return lds_addr(smem) + L_C + (ev >> 2) * 256 + (ev & 3) * 4; }
DI void ev_set(char* smem, int ev, int wq, int lane) {
  if (lane == 0) *(volatile LDS_AS unsigned char*)(size_t)(ev_addr(smem, ev) + wq) = 1;
}
DI void ev_signal(char* smem, int ev, int wq, int lane) {
  asm volatile("s_waitcnt lgkmcnt(0)" ::: "memory");
  ev_set(smem, ev, wq, lane);
  asm volatile("" ::: "memory");
}
DI void ev_wait(char* smem, int ev) {
  asm volatile("" ::: "memory");
#pragma unroll 1
  for (int spin = 0; spin < (1 << 22); ++spin) {
    unsigned v = *(volatile LDS_AS unsigned*)(size_t)ev_addr(smem, ev);
    if (__builtin_amdgcn_readfirstlane(v) == 0x01010101u) break;
    __builtin_amdgcn_s_sleep(2);
  }
  asm volatile("" ::: "memory");
}
DI void ev_signal_all(char* smem, int ev, int lane) {
  asm volatile("" ::: "memory");
  if (lane == 0) *(volatile LDS_AS unsigned*)(size_t)ev_addr(smem, ev) = 0x01010101u;
  asm volatile("" ::: "memory");
}

extern "C" __global__ __launch_bounds__(512, 2) void k2_mega(const bf16* __restrict__ x, const bf16* __restrict__ w2,
                                                  const bf16* __restrict__ w3, const bf16* __restrict__ w4,
                                                  const bf16* __restrict__ wo, char* __restrict__ ws,
                                                  bf16* __restrict__ out) {
  __shared__ __attribute__((aligned(16))) char smem[163840];
  const int tid = threadIdx.x, lane = tid & 63;
  const int w8 = __builtin_amdgcn_readfirstlane(tid >> 6);
  const int grp = w8 >> 2, w = w8 & 3;
  const int xcd = blockIdx.x & 7, idx = blockIdx.x >> 3;
  STAMP_DECL STAMP(0);
  const unsigned l0 = lds_addr(smem);
  const bool has_gemm = idx < 28;
  const int rt64 = 4 * xcd + (idx & 3), c7 = idx >> 2;
  const int panel = 2 * xcd + (idx & 1), c14 = idx >> 1;
  const int qb = idx >> 1, head = 2 * xcd + (idx & 1);
  const int g = lane >> 4, q = (lane >> 2) & 3, p = lane & 3, r16 = lane & 15, r8 = lane >> 3;
  const rsrc_t rs = __builtin_amdgcn_make_buffer_rsrc(ws, 0, (int)WS_USED, 0x00020000);
  const int btr = (4 * g + q) * 128 + (swz(4 * g + q) << 5) + p * 8;
  const unsigned cE = src_chunk(r8, lane) * 16, cO = src_chunk(r8 + 8, lane) * 16;
  auto loadW = [&](const char* base, int ld, int g0, int n, unsigned dst) {
    const unsigned oE = r8 * ld + cE, oO = (r8 + 8) * ld + cO;
#pragma unroll 1
    for (int m = g0; m < g0 + n; ++m) {
      glds16(base + (size_t)m * 16 * ld, oE, dst + m * 2048);
      glds16(base + (size_t)m * 16 * ld, oO, dst + m * 2048 + 1024);
    }
  };
  const char* w3b = (const char*)(w3 + c14 * 128);
  bf16x8 bp[7][4];
  const unsigned boff = OFF_BT + (c7 * 72 + w) * 4096 + lane * 16;
  auto ldB = [&](bf16x8 (&bf)[4], int k4) {
#pragma unroll
    for (int j = 0; j < 4; ++j) bf[j] = as_frag(ld16(rs, (boff ^ ((w8 & 1) << 11) ^ (j << 10)) + k4 * 16384));
  };
  if (tid < 16) *(unsigned*)(smem + L_C + (tid >> 2) * 256 + (tid & 3) * 4) = 0u;
  const int cons3 = ((2 * lane + ((rt64 >> 1) & 1)) << 3) | xcd;
  const int cons4 = ((4 * (lane >> 1) + 2 * (qb & 1) + (lane & 1)) << 3) | (qb >> 1);

  if (grp == 0) {
    if (!has_gemm) {
      BAR();
      unsigned sink = 0;
      const int wv = (idx - 28) * 4 + w;
      __builtin_amdgcn_s_sleep(110);
#pragma unroll 1
      for (int m = 0; m < 16; ++m) {
        const int c = wv + 16 * m;
        touch8k(sink, ws + OFF_BT, c < 252 ? c : 0, lane);
        WAIT_V(1);
      }
      WAIT_V(0); touch_keep(sink);
      return;
    }
    __builtin_amdgcn_s_setprio(2);
    loadW((const char*)(w2 + c7 * 64), D_ * 2, 7 * w, 7, l0);
    {
      const char* bx = (const char*)(x + (size_t)(rt64 * 64 + 16 * w) * D_ + c7 * 64);
#pragma unroll
      for (int i = 0; i < 2; ++i) glds16(bx, ((8 * i + r8) * D_ + (((lane & 7) ^ r8) << 3)) * 2, l0 + L_X + w * 2048 + i * 1024);
    }
    u32x4 ha[14];
#pragma unroll
    for (int ks = 0; ks < 14; ++ks) ha[ks] = ld16(rs, OFF_HS + (((rt64 * 4 + w) * 14 + ks) * 64 + lane) * 16);
    BAR();
    WAIT_V(14);
    ev_signal(smem, EV_W2, w, lane);
    ev_wait(smem, EV_W2);
    STAMP(1);
    {
      f32x4 acc[4] = {};
      asm volatile("" : "+v"(acc[0]), "+v"(acc[1]), "+v"(acc[2]), "+v"(acc[3]));
      bf16x8 bq[3][4];
      auto rdB = [&](int ks, int j) {
        const char* bp = smem + ks * 4096 + (btr ^ (j << 5));
        bq[ks % 3][j] = ld_tr2(bp, bp + 2048);
      };
#pragma unroll
      for (int j = 0; j < 4; ++j) { rdB(0, j); rdB(1, j); }
      const int trow = 16 * w + r16;
      u32x2 xr[4];
      SB();
#pragma unroll
      for (int ks = 0; ks < 14; ++ks) {
#pragma unroll
        for (int j = 0; j < 4; ++j) {
          MFMA16_IP(bq[ks % 3][j], ha[ks], acc[j]);
          SB();
          if (ks + 2 < 14) rdB(ks + 2, j);
          if (ks == 12) xr[j] = *(const u32x2*)(smem + L_X + trow * 128 + (((2 * j + (g >> 1)) ^ (trow & 7)) << 4) + (g & 1) * 8);
          SB();
        }
      }
      MFMA_DRAIN();
      SB();
      STAMP(11);
      float sum = 0.f, sq = 0.f;
      u32x4 x1k[2];
#pragma unroll
      for (int j = 0; j < 4; ++j) {
        unsigned lo = pack2(acc[j][0] + bf_lo(xr[j][0]), acc[j][1] + bf_hi(xr[j][0]));
        unsigned hi = pack2(acc[j][2] + bf_lo(xr[j][1]), acc[j][3] + bf_hi(xr[j][1]));
        x1k[j >> 1][2 * (j & 1)] = lo; x1k[j >> 1][2 * (j & 1) + 1] = hi;
        float e0 = bf_lo(lo), e1 = bf_hi(lo), e2 = bf_lo(hi), e3 = bf_hi(hi);
        sum += (e0 + e1) + (e2 + e3);
        sq += (e0 * e0 + e1 * e1) + (e2 * e2 + e3 * e3);
      }
#pragma unroll
      for (int jj = 0; jj < 2; ++jj) st16_sc1(rs, OFF_X1 + (((rt64 * 4 + w) * 14 + 2 * c7 + jj) * 64 + lane) * 16, x1k[jj]);
      u32x2 t = {__float_as_uint(xsum4(sum)), __float_as_uint(xsum4(sq))};
      if (g == 0) st8_sc1(rs, OFF_ST + (rt64 * 4 + w) * 1024 + (c7 >> 1) * 256 + r16 * 16 + (c7 & 1) * 8, t);
    }
    ev_signal(smem, EV_P2, w, lane);
    WAIT_V(0);
    post_flags(ws, 2 * (rt64 & 1) + (w >> 1), cons3, 14, c7 * 2 + (w & 1), lane);
    STAMP(2);
    ev_wait(smem, EV_P2);
    loadW(w3b, F_ * 2, 7 * w, 7, l0);
    loadW(w3b + 128, F_ * 2, 24 + w, 1, l0 + L_H1);
    f32x4 acc[2][8] = {};
#pragma unroll
    for (int j = 0; j < 8; ++j) asm volatile("" : "+v"(acc[0][j]), "+v"(acc[1][j]));
    unsigned hb[8];
#pragma unroll
    for (int j = 0; j < 8; ++j) hb[j] = opaque(l0 + (j >> 2) * L_H1 + (btr ^ ((j & 3) << 5)));
    const unsigned aoff = opaque(OFF_X1 + ((panel * 8 + 2 * w) * 14 * 64 + lane) * 16);
    WAIT_V(0);
    ev_signal(smem, EV_W3A, w, lane);
    STAMP(3);
    wait_flags(ws, w, 14, lane);
    STAMP(4);
    {
      u32x4 a[2][14];
      auto ldA = [&](int ks) {
        a[0][ks] = ld16_sc1(rs, aoff + ks * 1024);
        a[1][ks] = ld16_sc1(rs, aoff + 14336 + ks * 1024);
      };
#pragma unroll
      for (int ks = 0; ks < 6; ++ks) ldA(ks);
      ev_wait(smem, EV_W3A);
      ev_wait(smem, EV_ATT);
      STAMP(5);
      u32x4 stv[2];
      float g1[2];
      f32x4 c4[8];
      bf16x8 bq[8];
      auto rdB = [&](int n) {
        const int ks = n >> 3, j = n & 7;
        const unsigned bp = hb[j] + ks * 4096;
        return ld_tr2a(bp, bp + 2048);
      };
#pragma unroll
      for (int n = 0; n < 6; ++n) bq[n] = rdB(n);
      SB();
#pragma unroll
      for (int ks = 0; ks < 14; ++ks) {
        if (ks == 6) ev_set(smem, EV_X1, w, lane);
#pragma unroll
        for (int j = 0; j < 8; ++j) {
          const int n = ks * 8 + j;
          MFMA16_IP(bq[j], a[0][ks], acc[0][j]);
          MFMA16_IP(bq[j], a[1][ks], acc[1][j]);
          SB();
          if (n + 6 < 112) bq[(j + 6) & 7] = rdB(n + 6);
          if ((j == 1 || j == 5) && ks < 4) ldA(2 * ks + 6 + (j >> 2));
          if (ks == 4 && j < 2) {
            stv[j] = ld16_sc1(rs, OFF_ST + (panel * 8 + 2 * w + j) * 1024 + lane * 16);
            g1[j] = ((const float*)(ws + OFF_G))[(panel * 128 + 32 * w + 16 * j + r16) * 2 + 1];
          }
          if (ks == 5) c4[j] = *(const f32x4*)(ws + OFF_CS + (size_t)(c14 * 128 + 16 * j + 4 * g) * 4);
          SB();
        }
      }
      MFMA_DRAIN();
      SB();
      ev_signal(smem, EV_P3, w, lane);
      STAMP(6);
#pragma unroll
      for (int rf = 0; rf < 2; ++rf) {
        float s1 = __uint_as_float(stv[rf][0]), s2 = __uint_as_float(stv[rf][1]);
        if (g < 3) { s1 += __uint_as_float(stv[rf][2]); s2 += __uint_as_float(stv[rf][3]); }
        s1 = xsum4(s1); s2 = xsum4(s2);
        float mean = s1 * (1.f / D_);
        float var = fmaxf(s2 * (1.f / D_) - mean * mean, 0.f);
        float a_ = __builtin_amdgcn_rsqf(var + 1e-5f);
        float k1 = GC1 * a_, k3 = GC3 * a_ * a_ * a_, inv = __builtin_amdgcn_rcpf(fmaxf(a_ * g1[rf], 1e-30f));
        const f32x2 nm = {-mean, -mean};
#pragma unroll
        for (int jj = 0; jj < 4; ++jj) {
          u32x4 v;
#pragma unroll
          for (int h = 0; h < 2; ++h) {
            const int j = 2 * jj + h;
            f32x2 z0 = __builtin_elementwise_fma(nm, f32x2{c4[j][0], c4[j][1]}, f32x2{acc[rf][j][0], acc[rf][j][1]});
            f32x2 z1 = __builtin_elementwise_fma(nm, f32x2{c4[j][2], c4[j][3]}, f32x2{acc[rf][j][2], acc[rf][j][3]});
            v[2 * h] = pack2v(gelu2(z0, k1, k3, inv));
            v[2 * h + 1] = pack2v(gelu2(z1, k1, k3, inv));
          }
          st16_sc1(rs, OFF_AC + (((panel * 8 + 2 * w + rf) * 72 + 16 + c14 * 4 + jj) * 64 + lane) * 16, v);
        }
      }
    }
    STAMP(7);
    WAIT_V(0);
    STAMP(12);
    post_flags(ws, c14 < 7 ? 6 : 5, ((4 * lane + 2 * (panel & 1) + (w >> 1)) << 3) | xcd, 7, (c14 < 7 ? c14 : c14 - 7) * 2 + (w & 1), lane);
    __builtin_amdgcn_s_setprio(0);
    ev_wait(smem, EV_BA);
#pragma unroll
    for (int m = 0; m < 7; ++m) {
      const int n = 7 * w + m;
      const char* sl = smem + n * 4096 + lane * 16;
#pragma unroll
      for (int j = 0; j < 4; ++j) bp[m][j] = ld_frag(sl + ((j << 10) ^ ((w8 & 1) << 11)));
    }
    ev_signal(smem, EV_BAR, w, lane);
    STAMP(8);

  } else {
    const int gb = (7 * head) >> 1;
    const int qw = qb * 128 + 32 * w;
    unsigned vk[6], vq[2];
    {
#pragma unroll
      for (int m = 0; m < 6; ++m) {
        int kt = qb * 8 - 16 + 6 * w + m;
        kt = kt < 0 ? 0 : kt;
        vk[m] = opaque(kt * 43008 + (56 + gb) * 256 + lane * 16);
      }
#pragma unroll
      for (int h = 0; h < 2; ++h) vq[h] = opaque((((qb * 8 + 2 * w + h) * 168 + gb) * 16 + lane) * 16);
    }
    const unsigned vx = opaque(56 * 256 + (((lane & 16) >> 1) << 4));
    const unsigned lkv = l0 + L_C + w * 6144;
    const unsigned long long mask0 = w == 0 ? 0xfffefffefffefffeull : ~0ull;
    BAR();
    __builtin_amdgcn_s_setprio(3);
    asm volatile("s_mov_b32 m0, %2\n\ts_mov_b64 exec, %3\n\tglobal_load_lds_dwordx4 %0, %1\n\ts_mov_b64 exec, -1" ::"v"(vk[0]),
                 "s"(ws + OFF_QKV), "s"(lkv), "s"(mask0) : "memory");
    glds16c<1024>(ws + OFF_QKV, vk[1], lkv); glds16c<2048>(ws + OFF_QKV, vk[2], lkv); glds16c<3072>(ws + OFF_QKV, vk[3], lkv);
    glds16c<4096>(ws + OFF_QKV, vk[4], lkv); glds16c<5120>(ws + OFF_QKV, vk[5], lkv);
    u32x4 qv[2];
#pragma unroll
    for (int h = 0; h < 2; ++h)
      asm volatile("global_load_dwordx4 %0, %1, %2" : "=v"(qv[h]) : "v"(vq[h]), "s"(ws + OFF_QKV) : "memory");
#define VLOAD(m) glds16c<24576 + (m) * 1024>(ws + OFF_QKV, (vk[m] ^ (vx & 128)) + (vx & ~128u), lkv)
    VLOAD(0); VLOAD(1); VLOAD(2); VLOAD(3); VLOAD(4); VLOAD(5);
    WAIT_V(6);
    asm volatile("" : "+v"(qv[0]), "+v"(qv[1])::"memory");
    ev_signal(smem, EV_KV, w, lane);
    if (has_gemm) {
      const unsigned o0 = (96 * w + r8) * (F_ * 2) + cE, o1 = (96 * w + r8) * (F_ * 2) + cO;
      const unsigned ld = l0 + L_H1 + w * 12288;
#pragma unroll
      for (int m = 0; m < 12; ++m) glds16(w3b + 128, ((m & 1) ? o1 : o0) + m * 8 * F_ * 2, ld + m * 1024);
    }
    ev_wait(smem, EV_KV);
    STAMP(1);
    const char* sK = smem + L_C;
    const char* sV = smem + L_C + 24576;
    const bool odd = head & 1;
    const bool z01 = odd && g == 0, z23 = !odd && g == 3;
    bf16x8 qf[2];
#pragma unroll
    for (int h = 0; h < 2; ++h) {
      qv[h][0] = z01 ? 0u : qv[h][0]; qv[h][1] = z01 ? 0u : qv[h][1];
      qv[h][2] = z23 ? 0u : qv[h][2]; qv[h][3] = z23 ? 0u : qv[h][3];
      qf[h] = as_frag(qv[h]);
    }
    const float NEG = -__builtin_inff();
    constexpr float SC = 0.1889822365046136f * 1.4426950408889634f;
    f32x4 s[2][17];
    float mx[2] = {NEG, NEG};
    auto max4 = [&](float m, const f32x4& v) {
      return __builtin_fmaxf(__builtin_fmaxf(__builtin_fmaxf(__builtin_fmaxf(m, v[0]), v[1]), v[2]), v[3]);
    };
    auto finish = [&](int h, int t) {
      if (t == 0 || t == 16) {
#pragma unroll
        for (int i = 0; i < 4; ++i) {
          const int b = 4 * g + i;
          s[h][t][i] = ((b > r16) == (t == 0)) ? s[h][t][i] : NEG;
        }
      }
      mx[h] = max4(mx[h], s[h][t]);
    };
    {
      bf16x8 kf[6];
#pragma unroll
      for (int t = 0; t < 6; ++t) kf[t] = ld_frag(sK + (2 * w + t) * 1024 + lane * 16);
      SB();
#pragma unroll
      for (int t = 0; t < 20; ++t) {
        if (t < 18) {
          f32x4 z = {};
          if (t < 17) s[0][t] = MFMA16(kf[t % 6], qf[0], z);
          if (t > 0) s[1][t - 1] = MFMA16(kf[t % 6], qf[1], z);
          if (t + 6 < 18) kf[t % 6] = ld_frag(sK + (2 * w + t + 6) * 1024 + lane * 16);
        }
        if (t >= 2) {
          if (t - 2 < 17) finish(0, t - 2);
          if (t - 2 > 0) finish(1, t - 3);
        }
        SB();
      }
    }
    if (qw < 256) {
#pragma unroll
      for (int h = 0; h < 2; ++h) {
        const int tstart = 16 - (qw >> 4) - h;
        mx[h] = NEG;
#pragma unroll
        for (int t = 0; t < 17; ++t) {
          if (t < tstart) s[h][t] = f32x4{NEG, NEG, NEG, NEG};
          mx[h] = max4(mx[h], s[h][t]);
        }
      }
    }
    const float mc[2] = {-xmax4(mx[0]) * SC, -xmax4(mx[1]) * SC};
    STAMP(2);
    WAIT_V(0);
    ev_signal(smem, EV_W3B, w, lane);
    ev_wait(smem, EV_W3B);
    float gt[2];
#pragma unroll
    for (int h = 0; h < 2; ++h) gt[h] = ((const float*)(ws + OFF_G))[(qw + 16 * h + r16) * 2];
    STAMP(3);
    f32x4 o[2][2] = {}, lsum[2] = {};
    bf16x8 ones;
#pragma unroll
    for (int i = 0; i < 8; ++i) ones[i] = (bf16)1.0f;
    auto pexp = [&](const f32x4& sv, float m, unsigned& lo, unsigned& hi) {
      f32x2 a = __builtin_elementwise_fma(f32x2{sv[0], sv[1]}, f32x2{SC, SC}, f32x2{m, m});
      f32x2 b = __builtin_elementwise_fma(f32x2{sv[2], sv[3]}, f32x2{SC, SC}, f32x2{m, m});
      lo = pack2(__builtin_amdgcn_exp2f(a[0]), __builtin_amdgcn_exp2f(a[1]));
      hi = pack2(__builtin_amdgcn_exp2f(b[0]), __builtin_amdgcn_exp2f(b[1]));
    };
    const int voff0 = (p >> 1) * 256 + (((4 * g + q) ^ ((p >> 1) << 3)) << 4) + (p & 1) * 8;
    bf16x8 vf[2][2], pf[2][2];
#pragma unroll
    for (int kk = 0; kk < 10; ++kk) {
      if (kk < 9) {
        const char* vb = sV + (2 * w + 2 * kk) * 1024 + voff0;
        vf[kk & 1][0] = ld_tr2(vb, vb + 1024);
        vf[kk & 1][1] = ld_tr2(vb + 512, vb + 1536);
#pragma unroll
        for (int h = 0; h < 2; ++h) {
          unsigned p0 = 0, p1 = 0, p2 = 0, p3 = 0;
          const int t0 = 2 * kk - h, t1 = t0 + 1;
          if (t0 >= 0) pexp(s[h][t0 < 0 ? 0 : t0], mc[h], p0, p1);
          if (t1 <= 16) pexp(s[h][t1 > 16 ? 16 : t1], mc[h], p2, p3);
          pf[kk & 1][h] = as_frag(u32x4{p0, p1, p2, p3});
        }
      }
      if (kk > 0) {
        const int c = (kk - 1) & 1;
#pragma unroll
        for (int h = 0; h < 2; ++h) {
          o[h][0] = MFMA16(vf[c][0], pf[c][h], o[h][0]);
          o[h][1] = MFMA16(vf[c][1], pf[c][h], o[h][1]);
          lsum[h] = MFMA16(ones, pf[c][h], lsum[h]);
        }
      }
      SGB(0x100, 4);
#pragma unroll
      for (int m = 0; m < 6; ++m) { SGB(0x8, 1); SGB(0x402, 5); }
      SB();
    }
    __builtin_amdgcn_s_setprio(3);
    STAMP(4);
    u32x4 ov[2];
#pragma unroll
    for (int h = 0; h < 2; ++h) {
      float sc = gt[h] * __builtin_amdgcn_rcpf(lsum[h][0]);
      ov[h][0] = z01 ? 0u : pack2(o[h][0][0] * sc, o[h][0][1] * sc);
      ov[h][1] = z01 ? 0u : pack2(o[h][0][2] * sc, o[h][0][3] * sc);
      ov[h][2] = z23 ? 0u : pack2(o[h][1][0] * sc, o[h][1][1] * sc);
      ov[h][3] = z23 ? 0u : pack2(o[h][1][2] * sc, o[h][1][3] * sc);
    }
    asm volatile("" : "+v"(ov[0]), "+v"(ov[1]));
    ev_signal(smem, EV_ATT, w, lane);
#pragma unroll
    for (int h = 0; h < 2; ++h) st16_sc1(rs, OFF_AC + (((qb * 8 + 2 * w + h) * 72 + head) * 64 + lane) * 16, ov[h]);
    WAIT_V(0);
    ev_signal(smem, EV_O, w, lane);
    if (w == 0) {
      ev_wait(smem, EV_O);
      post_flags(ws, 4, cons4, 14, head, lane);
    }
    if (!has_gemm) return;
  }
  {
    unsigned aoff[4];
#pragma unroll
    for (int rt = 0; rt < 4; ++rt) aoff[rt] = OFF_AC + (((rt64 * 4 + (rt ^ (w8 >> 1))) * 72 + w) * 64 + lane) * 16;
    f32x4 acc[4][4] = {};
    u32x4 a[3][4];
    auto ldA = [&](int buf, int k4) {
#pragma unroll
      for (int rt = 0; rt < 4; ++rt) a[buf][rt] = ld16_sc1(rs, aoff[rt] + k4 * 4096);
    };
    auto mma = [&](const u32x4 (&af)[4], const bf16x8 (&bf)[4]) {
#pragma unroll
      for (int rt = 0; rt < 4; ++rt)
#pragma unroll
        for (int j = 0; j < 4; ++j) acc[rt][j] = MFMA16(bf[j], as_frag(af[rt]), acc[rt][j]);
    };
    if (grp == 1) {
      bf16x8 bo[4];
      STAMP(5);
      ev_wait(smem, EV_X1);
      ldB(bp[6], 0); ldB(bo, 1);
#pragma unroll
      for (int m = 0; m < 6; ++m) ldB(bp[m], 4 + m);
      STAMP(6);
#pragma unroll
      for (int m = 0; m < 7; ++m)
#pragma unroll
        for (int j = 0; j < 4; ++j) asm volatile("" : "+v"(bp[m][j]));
#pragma unroll
      for (int j = 0; j < 4; ++j) asm volatile("" : "+v"(bo[j]));
      ev_wait(smem, EV_P3);
#pragma unroll
      for (int m = 0; m < 7; ++m) {
        const int n = 7 * w + m;
        const unsigned dst = l0 + n * 4096;
        const char* src = ws + OFF_BT + (size_t)(c7 * 72 + 44 + w + 4 * m) * 4096;
        glds16<0>(src, lane * 16, dst); glds16<1024>(src, lane * 16, dst);
        glds16<2048>(src, lane * 16, dst); glds16<3072>(src, lane * 16, dst);
        WAIT_V(8);
      }
      if (w == 0) {
        wait_flags(ws, 4, 16, lane);
        ev_signal_all(smem, EV_OF, lane);
      } else {
        ev_wait(smem, EV_OF);
      }
      ldA(0, 0); ldA(1, 1);
      WAIT_V(8);
      ev_signal(smem, EV_BA, w, lane);
      STAMP(7);
      __builtin_amdgcn_s_setprio(0);
      SB();
      mma(a[0], bp[6]);
      SB();
      ldB(bp[6], 2); ldA(0, 2);
      SB();
      mma(a[1], bo);
      SB();
      ldB(bo, 3); ldA(1, 3);
      SB();
      mma(a[0], bp[6]);
      SB();
      ldB(bp[6], 10);
      SB();
      mma(a[1], bo);
      SB();
      STAMP(8);
    }
    if (w == 0) {
      wait_flags(ws, 5 + grp, 14, lane);
      ev_signal_all(smem, EV_H3 + grp, lane);
    } else {
      ev_wait(smem, EV_H3 + grp);
    }
    STAMP(9);
    const int k0 = grp ? 4 : 11;
#pragma unroll
    for (int m = 0; m < 3; ++m) ldA(m, k0 + m);
    const int tt = rt64 * 4 + (w8 >> 1);
    u32x4 x1v;
    float g1o;
    SB();
#pragma unroll
    for (int m = 0; m < 7; ++m) {
      mma(a[m % 3], bp[m]);
      SB();
      if (m + 3 < 7) ldA(m % 3, k0 + m + 3);
      if (m == 4) {
        x1v = ld16_sc1(rs, OFF_X1 + ((tt * 14 + 2 * c7 + (w8 & 1)) * 64 + lane) * 16);
        g1o = ((const float*)(ws + OFF_G))[(tt * 16 + r16) * 2 + 1];
      }
      SB();
    }
    STAMP(10);
    ev_wait(smem, EV_BAR);
    auto exa = [&](int dst) { return (f32x4*)(smem + dst * 14336) + lane; };
#pragma unroll
    for (int pos = 1; pos < 8; ++pos)
#pragma unroll
      for (int h = 0; h < 2; ++h) exa(w8 ^ pos)[((pos - 1) * 2 + h) * 64] = acc[pos >> 1][2 * (pos & 1) + h];
    BAR();
    STAMP(13);
    unsigned pk[2][2];
#pragma unroll
    for (int h = 0; h < 2; ++h) {
      f32x4 r = acc[0][h];
#pragma unroll
      for (int sidx = 0; sidx < 7; ++sidx) r += exa(w8)[(sidx * 2 + h) * 64];
      pk[h][0] = pack2(__builtin_fmaf(g1o, bf_lo(x1v[2 * h]), r[0]), __builtin_fmaf(g1o, bf_hi(x1v[2 * h]), r[1]));
      pk[h][1] = pack2(__builtin_fmaf(g1o, bf_lo(x1v[2 * h + 1]), r[2]), __builtin_fmaf(g1o, bf_hi(x1v[2 * h + 1]), r[3]));
    }
    auto s0 = __builtin_amdgcn_permlane16_swap(pk[0][0], pk[1][0], false, false);
    auto s1 = __builtin_amdgcn_permlane16_swap(pk[0][1], pk[1][1], false, false);
    u32x4 v = {s0[0], s1[0], s0[1], s1[1]};
    const int gl = (g >> 1) | ((g & 1) << 1);
    *(u32x4*)(out + (size_t)(tt * 16 + r16) * D_ + c7 * 64 + 32 * (w8 & 1) + 8 * gl) = v;
  }
  STAMP_FLUSH(ws, 1);
}

extern "C" void kernel_launch(void* const* d_in, const int* in_sizes, int n_in, void* d_out, int out_size, void* d_ws,
                              size_t ws_size, hipStream_t stream) {
  const bf16* x = (const bf16*)d_in[0];
  const bf16* wqkv = (const bf16*)d_in[1];
  const bf16* wo = (const bf16*)d_in[2];
  const bf16* w1 = (const bf16*)d_in[4];
  const bf16* w2 = (const bf16*)d_in[5];
  const bf16* w3 = (const bf16*)d_in[6];
  const bf16* w4 = (const bf16*)d_in[7];
  const bf16* gw = (const bf16*)d_in[8];
  char* ws = (char*)d_ws;
  k1_qkv_h<<<256, 384, 0, stream>>>(x, wqkv, w1, w2, w3, w4, wo, gw, ws);
  k2_mega<<<256, 512, 0, stream>>>(x, w2, w3, w4, wo, ws, (bf16*)d_out);
}
```
